```python
import torch

def _softmax(x, axis=-1):
    return torch.softmax(x, dim=axis)
def _gelu(x):
    return torch.nn.functional.gelu(x, approximate="tanh")
def _silu(x):
    return torch.nn.functional.silu(x)
def _sigmoid(x):
    return torch.sigmoid(x)
def _layernorm(x, axis=-1, eps=1e-5):
    m = x.mean(dim=axis, keepdim=True)
    v = x.var(dim=axis, keepdim=True, unbiased=False)
    return (x - m) / torch.sqrt(v + eps)
def _scatter_add(tgt, idx, src, axis=0):
    return tgt.clone().index_add_(axis, idx, src)
def _segment_sum(src, seg, n):
    out = torch.zeros((n,) + src.shape[1:], dtype=src.dtype)
    return out.index_add_(0, seg, src)
def _pad(x, pw):
    flat = tuple(v for pair in reversed(pw) for v in pair)
    return torch.nn.functional.pad(x, flat)

kernel_name = 'gated_mix__mha_block__glu_gated_geglu__prompt'

def setup_inputs():

    _w = (lambda a: (lambda t: t.to(torch.bfloat16) if t.is_floating_point() else t)(torch.as_tensor(a)))
    def _A_setup():
        B, S, D, H = 1, 2048, 512, 32
        return {
            "x": _w((lambda *s: torch.randn(*s, dtype=torch.bfloat16) * 0.05)(B, S, D)),
            "wqkv": _w((lambda *s: torch.randn(*s, dtype=torch.bfloat16) * 0.8)(D, 3 * D)),
            "wo": _w((lambda *s: torch.randn(*s, dtype=torch.bfloat16) * 0.05)(D, D)),
        }
    _A_in = _A_setup()

    def _B_setup():
        B, S, Dm, Df = 1, 2048, 512, 1376
        return {
            "x": _w((lambda *s: torch.randn(*s, dtype=torch.bfloat16) * 0.05)(B, S, Dm)),
            "w_gate": _w((lambda *s: torch.randn(*s, dtype=torch.bfloat16) * 3.2)(Dm, Df) * (Dm ** -0.5)),
            "w_up": _w((lambda *s: torch.randn(*s, dtype=torch.bfloat16) * 3.2)(Dm, Df) * (Dm ** -0.5)),
            "w_down": _w((lambda *s: torch.randn(*s, dtype=torch.bfloat16) * 3.2)(Df, Dm) * (Df ** -0.5)),
        }
    _B_in = _B_setup()

    return {
        "A_x": _A_in["x"],
        "A_wqkv": _A_in["wqkv"],
        "A_wo": _A_in["wo"],
        "B_x": _B_in["x"],
        "B_w_gate": _B_in["w_gate"],
        "B_w_up": _B_in["w_up"],
        "B_w_down": _B_in["w_down"],
        "gate_w": _w((lambda *s: torch.randn(*s, dtype=torch.bfloat16) * 0.05)(512) * 16.0),
    }

def reference(A_x, A_wqkv, A_wo, B_x, B_w_gate, B_w_up, B_w_down, gate_w):
    def _A_ref(x, wqkv, wo):
        B, S, D = x.shape
        H = 32
        Dh = D // H
        qkv = (lambda x, *d: x.permute(*d))((x @ wqkv).reshape(B, S, 3, H, Dh), 2, 0, 3, 1, 4)
        q, k, v = qkv[0], qkv[1], qkv[2]
        scores = torch.einsum("bhqd,bhkd->bhqk", q, k) * (Dh ** -0.5)
        p = _softmax(scores, axis=-1)
        o = torch.einsum("bhqk,bhkd->bhqd", p, v)
        return (lambda x, *d: x.permute(*d))(o, 0, 2, 1, 3).reshape(B, S, D) @ wo

    def _B_ref(x, w_gate, w_up, w_down):
        g = torch.einsum("bsd,df->bsf", x, w_gate)
        u = torch.einsum("bsd,df->bsf", x, w_up)
        h = _gelu(g) * u
        return torch.einsum("bsf,fd->bsd", h, w_down)

    x = A_x
    g = _sigmoid((x * gate_w).sum(-1, keepdims=True))
    a = _A_ref(x, A_wqkv, A_wo)
    b = _B_ref(x, B_w_gate, B_w_up, B_w_down)
    return g * a + (1 - g) * b
```

```cpp
#include <hip/hip_runtime.h>
#include <hip/hip_bf16.h>
#include <stdint.h>

typedef __attribute__((ext_vector_type(8))) short s16x8;
typedef __attribute__((ext_vector_type(4))) short s16x4;
typedef __attribute__((ext_vector_type(8))) __bf16 bf16x8;
typedef __attribute__((ext_vector_type(2))) __bf16 bf16x2;
typedef __attribute__((ext_vector_type(2))) float f32x2;
typedef __attribute__((ext_vector_type(16))) float f32x16;
typedef __attribute__((ext_vector_type(4))) unsigned u32x4;
typedef __attribute__((ext_vector_type(2))) unsigned u32x2;
typedef __attribute__((address_space(3))) char* lptr;
typedef __attribute__((address_space(3))) const char* lcptr;
typedef __amdgpu_buffer_rsrc_t srd_t;

#define DI __device__ __forceinline__
#define SBAR() __builtin_amdgcn_sched_barrier(0)

constexpr int S_ = 2048, D_ = 512, NH = 32, F_ = 1376;
constexpr int NKS = 32 + 86;
constexpr size_t OFF_Q = 0;
constexpr size_t OFF_K = 2u << 20;
constexpr size_t OFF_V = 4u << 20;
constexpr size_t OFF_ACT = 6u << 20;
constexpr size_t ACT_BYTES = (size_t)64 * NKS * 1024;
constexpr size_t OFF_G = 16u << 20;

DI f32x16 mfma32(s16x8 a, s16x8 b, f32x16 c) {
    return __builtin_amdgcn_mfma_f32_32x32x16_bf16(__builtin_bit_cast(bf16x8, a), __builtin_bit_cast(bf16x8, b), c, 0, 0, 0);
}
DI unsigned pack2(float a, float b) { f32x2 v = {a, b}; return __builtin_bit_cast(unsigned, __builtin_convertvector(v, bf16x2)); }
DI float bf2f(unsigned short v) { return __uint_as_float((unsigned)v << 16); }
DI srd_t make_srd(const void* p, unsigned bytes) { return __builtin_amdgcn_make_buffer_rsrc((void*)p, (short)0, (int)bytes, 0x00020000); }
DI void dma16(srd_t srd, unsigned voff, unsigned soff, unsigned ldsaddr) {
    asm volatile("s_mov_b32 m0, %3\n\ts_nop 0\n\tbuffer_load_dwordx4 %0, %1, %2 offen lds" :: "v"(voff), "s"(srd), "s"(soff), "s"(ldsaddr) : "memory");
}
DI s16x8 lds_b128(lcptr p) { return *(const __attribute__((address_space(3))) s16x8*)p; }
typedef short v4i16_t __attribute__((ext_vector_type(4)));
DI s16x4 lds_tr(lcptr p) { return __builtin_bit_cast(s16x4, __builtin_amdgcn_ds_read_tr16_b64_v4i16((__attribute__((address_space(3))) v4i16_t*)p)); }
DI s16x8 lds_tr2(lcptr p0, lcptr p1) { s16x4 a = lds_tr(p0), b = lds_tr(p1); return __builtin_shufflevector(a, b, 0, 1, 2, 3, 4, 5, 6, 7); }
#define WAITV(n) asm volatile("s_waitcnt vmcnt(" #n ")" ::: "memory")
template <int N> DI void wait_vm_bar() { asm volatile("s_waitcnt vmcnt(%0) lgkmcnt(0)\n\ts_barrier" :: "i"(N) : "memory"); }

DI u32x4 pack8(const f32x16& a, int r0, float sc) {
    u32x4 r; r[0] = pack2(a[r0] * sc, a[r0 + 1] * sc); r[1] = pack2(a[r0 + 2] * sc, a[r0 + 3] * sc);
    r[2] = pack2(a[r0 + 4] * sc, a[r0 + 5] * sc); r[3] = pack2(a[r0 + 6] * sc, a[r0 + 7] * sc); return r;
}

constexpr int P_NSLOT = 5, P_SLOT = 16384 + 3 * 4096;
__global__ __launch_bounds__(256) void k_qkv(const unsigned short* __restrict__ x, const unsigned short* __restrict__ w,
                                             const unsigned short* __restrict__ gw, char* __restrict__ ws) {
    __shared__ __attribute__((aligned(1024))) char lds[P_NSLOT * P_SLOT];
    const int lane = threadIdx.x & 63, wid = __builtin_amdgcn_readfirstlane(threadIdx.x >> 6);
    const int xcd = blockIdx.x & 7, idx = blockIdx.x >> 3, pi = xcd * 2 + (idx & 1), rb = idx >> 1;
    const int h = lane >> 5, m = lane & 31;
    const unsigned lds0 = (unsigned)(uintptr_t)(lptr)lds;
    const srd_t sx = make_srd(x, S_ * D_ * 2), sw = make_srd(w, D_ * 3 * D_ * 2);
    const int grow = rb * 128 + pi * 8 + wid * 2;
    const u32x4 gx0 = *(const u32x4*)(x + (size_t)grow * D_ + lane * 8), gx1 = *(const u32x4*)(x + (size_t)(grow + 1) * D_ + lane * 8);
    const u32x4 gwv = *(const u32x4*)(gw + lane * 8);
    unsigned vx[2];
#pragma unroll
    for (int t = 0; t < 2; ++t) { const int r = wid * 32 + t * 8 + (lane >> 3); vx[t] = (unsigned)((rb * 128 + r) * 1024 + (((lane & 7) ^ ((r >> 1) & 7)) << 4)); }
    const unsigned vw = (unsigned)((wid * 16 + (lane >> 2)) * 3072 + pi * 64 + (lane & 3) * 16);
    auto issue = [&](int c) {
        const unsigned sl = lds0 + (c % P_NSLOT) * P_SLOT;
#pragma unroll
        for (int t = 0; t < 4; ++t) dma16(sx, vx[t & 1], c * 128 + (t >> 1) * 16384, sl + wid * 4096 + t * 1024);
#pragma unroll
        for (int t = 0; t < 3; ++t) dma16(sw, vw, c * 64 * 3072 + t * 1024, sl + 16384 + t * 4096 + wid * 1024);
    };
#pragma unroll
    for (int c = 0; c < 5; ++c) issue(c);
    f32x16 aq = {}, ak = {}, av = {};
    const int xr = wid * 32 + m;
    unsigned xa[4];
#pragma unroll
    for (int ks = 0; ks < 4; ++ks) xa[ks] = (unsigned)(xr * 128 + (((2 * ks + h) ^ ((xr >> 1) & 7)) << 4));
    const unsigned wa = (unsigned)(16384 + (8 * h + ((lane & 15) >> 2)) * 64 + ((lane >> 4) & 1) * 32 + (lane & 3) * 8);
#pragma unroll
    for (int c = 0; c < 8; ++c) {
        if (c == 0) wait_vm_bar<28>(); else if (c <= 4) wait_vm_bar<21>(); else if (c == 5) wait_vm_bar<14>(); else if (c == 6) wait_vm_bar<7>(); else wait_vm_bar<0>();
        if (c >= 1 && c + 4 < 8) issue(c + 4);
        lcptr sl = (lcptr)lds + (c % P_NSLOT) * P_SLOT;
#pragma unroll
        for (int ks = 0; ks < 4; ++ks) {
            const s16x8 xf = lds_b128(sl + xa[ks]);
            const s16x8 wq = lds_tr2(sl + wa + ks * 1024, sl + wa + ks * 1024 + 256);
            const s16x8 wk = lds_tr2(sl + wa + 4096 + ks * 1024, sl + wa + 4096 + ks * 1024 + 256);
            const s16x8 wv = lds_tr2(sl + wa + 8192 + ks * 1024, sl + wa + 8192 + ks * 1024 + 256);
            aq = mfma32(wq, xf, aq); ak = mfma32(wk, xf, ak); av = mfma32(xf, wv, av);
        }
    }
    const int tile = rb * 4 + wid;
    constexpr float QS = 0.25f * 1.4426950408889634f;
#pragma unroll
    for (int hh = 0; hh < 2; ++hh) {
        const size_t o = ((size_t)((pi * 2 + hh) * 64 + tile) * 64 + lane) * 16;
        *(u32x4*)(ws + OFF_Q + o) = pack8(aq, hh * 8, QS);
        *(u32x4*)(ws + OFF_K + o) = pack8(ak, hh * 8, 1.f);
    }
#pragma unroll
    for (int s = 0; s < 2; ++s) {
        const size_t o = (size_t)((pi * 2 + (m >> 4)) * 64 + tile) * 1024 + s * 512 + h * 256 + (m & 15) * 16;
        *(u32x4*)(ws + OFF_V + o) = pack8(av, s * 8, 1.f);
    }
    float s0 = 0.f, s1 = 0.f;
#pragma unroll
    for (int i = 0; i < 4; ++i) {
        const float w0 = __uint_as_float(gwv[i] << 16), w1 = __uint_as_float(gwv[i] & 0xffff0000u);
        s0 += __uint_as_float(gx0[i] << 16) * w0 + __uint_as_float(gx0[i] & 0xffff0000u) * w1;
        s1 += __uint_as_float(gx1[i] << 16) * w0 + __uint_as_float(gx1[i] & 0xffff0000u) * w1;
    }
#pragma unroll
    for (int d = 1; d < 64; d <<= 1) { s0 += __shfl_xor(s0, d); s1 += __shfl_xor(s1, d); }
    if (lane < 2) { const float s = lane ? s1 : s0; ((float*)(ws + OFF_G))[grow + lane] = 1.f / (1.f + __expf(-s)); }
}

constexpr int G_NSLOT = 3, G_SLOT = 16384 + 6 * 4096;
DI float geglu(float g, float u) {
    constexpr float C0 = -2.f * 0.7978845608028654f * 1.4426950408889634f, C1 = C0 * 0.044715f;
    const float z = g * __builtin_fmaf(g * g, C1, C0);
    return g * u * __builtin_amdgcn_rcpf(1.f + __builtin_amdgcn_exp2f(z));
}
__global__ __launch_bounds__(256) void k_ffn1(const unsigned short* __restrict__ x, const unsigned short* __restrict__ wg,
                                              const unsigned short* __restrict__ wu, char* __restrict__ ws) {
    __shared__ __attribute__((aligned(1024))) char lds[G_NSLOT * G_SLOT];
    const int lane = threadIdx.x & 63, wid = __builtin_amdgcn_readfirstlane(threadIdx.x >> 6);
    const int xcd = blockIdx.x & 7, idx = blockIdx.x >> 3;
    const int rb = xcd * 2 + (idx & 1), fg = idx >> 1;
    const int h = lane >> 5, m = lane & 31;
    const unsigned lds0 = (unsigned)(uintptr_t)(lptr)lds;
    const srd_t sx = make_srd(x, S_ * D_ * 2), sg = make_srd(wg, D_ * F_ * 2), su = make_srd(wu, D_ * F_ * 2);
    unsigned vx[2];
#pragma unroll
    for (int t = 0; t < 2; ++t) { const int r = wid * 32 + t * 8 + (lane >> 3); vx[t] = (unsigned)((rb * 128 + r) * 1024 + (((lane & 7) ^ ((r >> 1) & 7)) << 4)); }
    const int nfb = (fg == 14) ? 1 : 3;
    const unsigned vw = (unsigned)((wid * 16 + (lane >> 2)) * (F_ * 2) + fg * 192 + (lane & 3) * 16);
    auto issue = [&](int c) {
        const unsigned sl = lds0 + (c % G_NSLOT) * G_SLOT;
#pragma unroll
        for (int t = 0; t < 4; ++t) dma16(sx, vx[t & 1], c * 128 + (t >> 1) * 16384, sl + wid * 4096 + t * 1024);
#pragma unroll
        for (int t = 0; t < 3; ++t) {
            const unsigned so = c * 64 * (F_ * 2) + (t < nfb ? t * 64 : 0);
            dma16(sg, vw, so, sl + 16384 + (2 * t) * 4096 + wid * 1024);
            dma16(su, vw, so, sl + 16384 + (2 * t + 1) * 4096 + wid * 1024);
        }
    };
    issue(0); issue(1); issue(2);
    f32x16 ag[3] = {}, au[3] = {};
    const int xr = wid * 32 + m;
    unsigned xa[4];
#pragma unroll
    for (int ks = 0; ks < 4; ++ks) xa[ks] = (unsigned)(xr * 128 + (((2 * ks + h) ^ ((xr >> 1) & 7)) << 4));
    const unsigned wa = (unsigned)(16384 + (8 * h + ((lane & 15) >> 2)) * 64 + ((lane >> 4) & 1) * 32 + (lane & 3) * 8);
#pragma unroll
    for (int c = 0; c < 8; ++c) {
        if (c == 0) wait_vm_bar<20>(); else if (c <= 6) wait_vm_bar<10>(); else wait_vm_bar<0>();
        if (c >= 1 && c + 2 < 8) issue(c + 2);
        lcptr sl = (lcptr)lds + (c % G_NSLOT) * G_SLOT;
#pragma unroll
        for (int ks = 0; ks < 4; ++ks) {
            const s16x8 xf = lds_b128(sl + xa[ks]);
#pragma unroll
            for (int t = 0; t < 3; ++t) {
                const s16x8 fg_ = lds_tr2(sl + wa + (2 * t) * 4096 + ks * 1024, sl + wa + (2 * t) * 4096 + ks * 1024 + 256);
                const s16x8 fu_ = lds_tr2(sl + wa + (2 * t + 1) * 4096 + ks * 1024, sl + wa + (2 * t + 1) * 4096 + ks * 1024 + 256);
                ag[t] = mfma32(fg_, xf, ag[t]); au[t] = mfma32(fu_, xf, au[t]);
            }
        }
    }
    const int tile = rb * 4 + wid;
#pragma unroll
    for (int t = 0; t < 3; ++t) {
        if (t < nfb) {
#pragma unroll
            for (int hh = 0; hh < 2; ++hh) {
                u32x4 r;
#pragma unroll
                for (int i = 0; i < 4; ++i) r[i] = pack2(geglu(ag[t][hh * 8 + 2 * i], au[t][hh * 8 + 2 * i]), geglu(ag[t][hh * 8 + 2 * i + 1], au[t][hh * 8 + 2 * i + 1]));
                *(u32x4*)(ws + OFF_ACT + ((size_t)(tile * NKS + 32 + (fg * 3 + t) * 2 + hh) * 64 + lane) * 16) = r;
            }
        }
    }
}

constexpr int A_SLOT = 8192, A_ONES = 3 * A_SLOT, A_FLAG = A_ONES + 4096, A_LDS = A_FLAG + 16;
DI float xhalf_max(float v) { auto r = __builtin_amdgcn_permlane32_swap(__float_as_uint(v), __float_as_uint(v), false, false); return fmaxf(__uint_as_float(r[0]), __uint_as_float(r[1])); }
DI float rowmax16(const f32x16& s) {
    float a = fmaxf(fmaxf(s[0], s[1]), s[2]), b = fmaxf(fmaxf(s[3], s[4]), s[5]);
    a = fmaxf(fmaxf(a, s[6]), s[7]); b = fmaxf(fmaxf(b, s[8]), s[9]); a = fmaxf(fmaxf(a, s[10]), s[11]); b = fmaxf(fmaxf(b, s[12]), s[13]);
    a = fmaxf(fmaxf(a, s[14]), s[15]); return xhalf_max(fmaxf(a, b));
}
#define EXP2(v) __builtin_amdgcn_exp2f(v)
__global__ __launch_bounds__(512) void k_attn(char* __restrict__ ws) {
    __shared__ __attribute__((aligned(1024))) char lds[A_LDS];
    const int lane = threadIdx.x & 63, wid = __builtin_amdgcn_readfirstlane(threadIdx.x >> 6);
    const int xcd = blockIdx.x & 7, idx = blockIdx.x >> 3, head = xcd * 4 + (idx & 3), qb = idx >> 2, qt = qb * 8 + wid;
    const int h = lane >> 5, r = lane & 31;
    const unsigned lds0 = (unsigned)(uintptr_t)(lptr)lds;
    const srd_t skv = make_srd(ws + OFF_K, 4u << 20);
    const unsigned vdma = lane * 16;
    const unsigned sbase = (wid < 4 ? 0u : (2u << 20)) + head * 65536 + (wid & 3) * 1024;
    const unsigned ldst = lds0 + (wid < 4 ? 0 : 4096) + (wid & 3) * 1024;
    auto issue = [&](int c, int slot) { dma16(skv, vdma, sbase + c * 4096, ldst + slot * A_SLOT); };
    issue(0, 0); issue(1, 1);
    const s16x8 qf = *(const s16x8*)(ws + OFF_Q + ((size_t)(head * 64 + qt) * 64 + lane) * 16);
    { u32x2 o2 = {0x3f803f80u, 0x3f803f80u}; *(__attribute__((address_space(3))) u32x2*)((lptr)lds + A_ONES + threadIdx.x * 8) = o2;
      if (threadIdx.x == 0) *(__attribute__((address_space(3))) unsigned*)((lptr)lds + A_FLAG) = 0u; }
    const unsigned kaddr = lane * 16;
    const bool isv = r < 16;
    const unsigned vlane = isv ? (unsigned)(4096 + h * 256 + r * 16) : (unsigned)(A_ONES + (r - 16) * 16);
    lcptr L = (lcptr)lds;

    wait_vm_bar<0>();
    f32x16 o = {}, sA, sB, cneg;
    s16x8 kf, v0, v1; u32x4 p0, p1;
    kf = lds_b128(L + kaddr);
    { f32x16 z = {}; sA = mfma32(kf, qf, z); const float m0 = rowmax16(sA);
#pragma unroll
      for (int i = 0; i < 16; ++i) cneg[i] = -m0;
#pragma unroll
      for (int i = 0; i < 16; ++i) sA[i] -= m0; }
    kf = lds_b128(L + kaddr + 1024);
    p0 = (u32x4){0, 0, 0, 0}; p1 = p0; v0 = lds_b128(L + vlane); v1 = v0;
#define PIN(x) asm volatile("" : "+v"(x))
#define STEP(SC, SN, KOFF, VOFF) do { \
        SN = mfma32(kf, qf, cneg); SBAR(); \
        kf = lds_b128(L + (KOFF)); \
        SC[0] = EXP2(SC[0]); SC[1] = EXP2(SC[1]); SC[2] = EXP2(SC[2]); SC[3] = EXP2(SC[3]); SBAR(); \
        o = mfma32(v0, __builtin_bit_cast(s16x8, p0), o); SBAR(); \
        v0 = lds_b128(L + (VOFF)); \
        SC[4] = EXP2(SC[4]); p0[0] = pack2(SC[0], SC[1]); SC[5] = EXP2(SC[5]); p0[1] = pack2(SC[2], SC[3]); SC[6] = EXP2(SC[6]); SC[7] = EXP2(SC[7]); SBAR(); \
        o = mfma32(v1, __builtin_bit_cast(s16x8, p1), o); SBAR(); \
        v1 = lds_b128(L + (VOFF) + 512); \
        SC[8] = EXP2(SC[8]); p0[2] = pack2(SC[4], SC[5]); SC[9] = EXP2(SC[9]); p0[3] = pack2(SC[6], SC[7]); \
        SC[10] = EXP2(SC[10]); SC[11] = EXP2(SC[11]); SBAR(); \
        SC[12] = EXP2(SC[12]); p1[0] = pack2(SC[8], SC[9]); SC[13] = EXP2(SC[13]); p1[1] = pack2(SC[10], SC[11]); \
        SC[14] = EXP2(SC[14]); SC[15] = EXP2(SC[15]); SBAR(); \
        p1[2] = pack2(SC[12], SC[13]); p1[3] = pack2(SC[14], SC[15]); SBAR(); \
    } while (0)
    int slot = 0;
    for (int c = 0; c < 16; ++c) {
        const int nslot = (slot == 2) ? 0 : slot + 1, fslot = (nslot == 2) ? 0 : nslot + 1;
        if (c + 2 < 16) issue(c + 2, fslot);
        const unsigned kb = kaddr + slot * A_SLOT, kn = kaddr + nslot * A_SLOT, vb = vlane + (isv ? slot * A_SLOT : 0);
        STEP(sA, sB, kb + 2048, vb);
        STEP(sB, sA, kb + 3072, vb + 1024);
        STEP(sA, sB, kn, vb + 2048);
        STEP(sB, sA, kn + 1024, vb + 3072);
        wait_vm_bar<0>();
        slot = nslot;
    }
    o = mfma32(v0, __builtin_bit_cast(s16x8, p0), o);
    o = mfma32(v1, __builtin_bit_cast(s16x8, p1), o);
    float l = o[8];
    bool bad = !(l <= 1.8446744e19f);
#pragma unroll
    for (int i = 0; i < 8; ++i) bad |= !(__builtin_fabsf(o[i]) <= 3.0e38f);
    if (__any(bad) && lane == 0) *(__attribute__((address_space(3))) unsigned*)((lptr)lds + A_FLAG) = 1u;
    asm volatile("s_waitcnt lgkmcnt(0)\n\ts_barrier" ::: "memory");
    const unsigned redo = __builtin_amdgcn_readfirstlane(*(volatile __attribute__((address_space(3))) unsigned*)((lptr)lds + A_FLAG));
    if (redo) {
        float mrun = -INFINITY;
#pragma unroll
        for (int i = 0; i < 16; ++i) o[i] = 0.f;
        for (int c = 0; c < 16; ++c) {
            asm volatile("s_waitcnt lgkmcnt(0)\n\ts_barrier" ::: "memory");
            issue(c, 0);
            wait_vm_bar<0>();
            for (int t = 0; t < 4; ++t) {
                f32x16 z = {};
                f32x16 s = mfma32(lds_b128(L + kaddr + t * 1024), qf, z);
                const float mn = fmaxf(mrun, rowmax16(s)), f = EXP2(mrun - mn);
                mrun = mn;
#pragma unroll
                for (int i = 0; i < 16; ++i) { o[i] *= f; s[i] = EXP2(s[i] - mn); }
                const u32x4 q0 = pack8(s, 0, 1.f), q1 = pack8(s, 8, 1.f);
                o = mfma32(lds_b128(L + vlane + t * 1024), __builtin_bit_cast(s16x8, q0), o);
                o = mfma32(lds_b128(L + vlane + t * 1024 + 512), __builtin_bit_cast(s16x8, q1), o);
            }
        }
        l = o[8];
    }
    const float rl = 1.f / l;
    *(u32x4*)(ws + OFF_ACT + ((size_t)(qt * NKS + head) * 64 + lane) * 16) = pack8(o, 0, rl);
}

constexpr int F_NSLOT = 4, F_SLOT = 32768;
__global__ __launch_bounds__(256) void k_final(const unsigned short* __restrict__ wo, const unsigned short* __restrict__ wd,
                                               const char* __restrict__ ws, unsigned short* __restrict__ out) {
    __shared__ __attribute__((aligned(1024))) char lds[F_NSLOT * F_SLOT];
    const int lane = threadIdx.x & 63, wid = __builtin_amdgcn_readfirstlane(threadIdx.x >> 6);
    const int xcd = blockIdx.x & 7, idx = blockIdx.x >> 3, mb = xcd * 4 + (idx >> 3), nb = idx & 7;
    const int h = lane >> 5, m = lane & 31, mi = wid & 1, ni = wid >> 1;
    const unsigned lds0 = (unsigned)(uintptr_t)(lptr)lds;
    const srd_t sa = make_srd(ws + OFF_ACT, (unsigned)ACT_BYTES), so = make_srd(wo, D_ * D_ * 2), sd = make_srd(wd, F_ * D_ * 2);
    const unsigned va = lane * 16;
    const unsigned sa0 = (unsigned)((mb * 2 + (wid & 1)) * NKS + (wid >> 1) * 4) * 1024;
    const unsigned vw = (unsigned)((wid * 32 + ((lane >> 2) & 7)) * 1024 + nb * 128 + (lane >> 5) * 64 + (lane & 3) * 16);
    auto issue = [&](int c) {
        const unsigned sl = lds0 + (c % F_NSLOT) * F_SLOT;
#pragma unroll
        for (int t = 0; t < 4; ++t) dma16(sa, va, sa0 + c * 8192 + t * 1024, sl + (wid & 1) * 8192 + ((wid >> 1) * 4 + t) * 1024);
#pragma unroll
        for (int t = 0; t < 4; ++t) {
            if (c < 4) dma16(so, vw, c * 131072 + t * 8192, sl + 16384 + (wid * 4 + t) * 1024);
            else dma16(sd, vw, (c - 4) * 131072 + t * 8192, sl + 16384 + (wid * 4 + t) * 1024);
        }
    };
    issue(0); issue(1); issue(2); issue(3);
    const float g = ((const float*)(ws + OFF_G))[mb * 64 + mi * 32 + m];
    f32x16 accA = {}, accB = {};
    const unsigned aa = mi * 8192 + lane * 16;
    const unsigned wa = (unsigned)(16384 + ni * 512 + (4 * h + ((lane & 15) >> 2)) * 64 + ((lane >> 4) & 1) * 32 + (lane & 3) * 8);
#pragma unroll
    for (int c = 0; c < 15; ++c) {
        if (c == 0) wait_vm_bar<24>(); else if (c <= 12) wait_vm_bar<16>(); else if (c == 13) wait_vm_bar<8>(); else wait_vm_bar<0>();
        if (c >= 1 && c + 3 < 15) issue(c + 3);
        lcptr sl = (lcptr)lds + (c % F_NSLOT) * F_SLOT;
#pragma unroll
        for (int ks = 0; ks < 8; ++ks) {
            if (c == 14 && ks >= 6) continue;
            const s16x8 af = lds_b128(sl + aa + ks * 1024);
            const s16x8 wf = lds_tr2(sl + wa + ks * 2048, sl + wa + ks * 2048 + 1024);
            if (c < 4) accA = mfma32(wf, af, accA); else accB = mfma32(wf, af, accB);
        }
    }
    const float g1 = 1.f - g;
    unsigned pk[8];
#pragma unroll
    for (int i = 0; i < 8; ++i) pk[i] = pack2(g * accA[2 * i] + g1 * accB[2 * i], g * accA[2 * i + 1] + g1 * accB[2 * i + 1]);
    unsigned short* orow = out + (size_t)(mb * 64 + mi * 32 + m) * D_ + nb * 64 + ni * 32 + h * 8;
#pragma unroll
    for (int i = 0; i < 2; ++i) {
        auto r0 = __builtin_amdgcn_permlane32_swap(pk[4 * i], pk[4 * i + 2], false, false);
        auto r1 = __builtin_amdgcn_permlane32_swap(pk[4 * i + 1], pk[4 * i + 3], false, false);
        u32x4 v = {r0[0], r1[0], r0[1], r1[1]};
        *(u32x4*)(orow + i * 16) = v;
    }
}

extern "C" void kernel_launch(void* const* d_in, const int* in_sizes, int n_in, void* d_out, int out_size, void* d_ws, size_t ws_size,
                              hipStream_t stream) {
    const unsigned short* x = (const unsigned short*)d_in[0];
    const unsigned short* wqkv = (const unsigned short*)d_in[1];
    const unsigned short* wo = (const unsigned short*)d_in[2];
    const unsigned short* wg = (const unsigned short*)d_in[4];
    const unsigned short* wu = (const unsigned short*)d_in[5];
    const unsigned short* wd = (const unsigned short*)d_in[6];
    const unsigned short* gw = (const unsigned short*)d_in[7];
    char* ws = (char*)d_ws;
    k_qkv<<<256, 256, 0, stream>>>(x, wqkv, gw, ws);
    k_ffn1<<<240, 256, 0, stream>>>(x, wg, wu, ws);
    k_attn<<<256, 512, 0, stream>>>(ws);
    k_final<<<256, 256, 0, stream>>>(wo, wd, ws, (unsigned short*)d_out);
}
```

```cpp
#define STAMPS
#include <hip/hip_runtime.h>
#include <hip/hip_bf16.h>
#include <stdint.h>

typedef __attribute__((ext_vector_type(8))) short s16x8;
typedef __attribute__((ext_vector_type(4))) short s16x4;
typedef __attribute__((ext_vector_type(8))) __bf16 bf16x8;
typedef __attribute__((ext_vector_type(2))) __bf16 bf16x2;
typedef __attribute__((ext_vector_type(2))) float f32x2;
typedef __attribute__((ext_vector_type(16))) float f32x16;
typedef __attribute__((ext_vector_type(4))) unsigned u32x4;
typedef __attribute__((ext_vector_type(2))) unsigned u32x2;
typedef __attribute__((address_space(3))) char* lptr;
typedef __attribute__((address_space(3))) const char* lcptr;
typedef __amdgpu_buffer_rsrc_t srd_t;

#define DI __device__ __forceinline__
#define STAMP(k, i) do { if (threadIdx.x == 0) { unsigned long long* p_ = (unsigned long long*)(ws_dbg + (64u << 20)) + ((size_t)(k) * 256 + blockIdx.x) * 16 + (i) * 2; p_[0] = __builtin_amdgcn_s_memrealtime(); p_[1] = __builtin_readcyclecounter(); } } while (0)
#define SBAR() __builtin_amdgcn_sched_barrier(0)

constexpr int S_ = 2048, D_ = 512, NH = 32, F_ = 1376;
constexpr int NKS = 32 + 86;
constexpr size_t OFF_Q = 0;
constexpr size_t OFF_K = 2u << 20;
constexpr size_t OFF_V = 4u << 20;
constexpr size_t OFF_ACT = 6u << 20;
constexpr size_t ACT_BYTES = (size_t)64 * NKS * 1024;
constexpr size_t OFF_G = 16u << 20;

DI f32x16 mfma32(s16x8 a, s16x8 b, f32x16 c) {
    return __builtin_amdgcn_mfma_f32_32x32x16_bf16(__builtin_bit_cast(bf16x8, a), __builtin_bit_cast(bf16x8, b), c, 0, 0, 0);
}
DI unsigned pack2(float a, float b) { f32x2 v = {a, b}; return __builtin_bit_cast(unsigned, __builtin_convertvector(v, bf16x2)); }
DI float bf2f(unsigned short v) { return __uint_as_float((unsigned)v << 16); }
DI srd_t make_srd(const void* p, unsigned bytes) { return __builtin_amdgcn_make_buffer_rsrc((void*)p, (short)0, (int)bytes, 0x00020000); }
DI void dma16(srd_t srd, unsigned voff, unsigned soff, unsigned ldsaddr) {
    asm volatile("s_mov_b32 m0, %3\n\ts_nop 0\n\tbuffer_load_dwordx4 %0, %1, %2 offen lds" :: "v"(voff), "s"(srd), "s"(soff), "s"(ldsaddr) : "memory");
}
DI s16x8 lds_b128(lcptr p) { return *(const __attribute__((address_space(3))) s16x8*)p; }
typedef short v4i16_t __attribute__((ext_vector_type(4)));
DI s16x4 lds_tr(lcptr p) { return __builtin_bit_cast(s16x4, __builtin_amdgcn_ds_read_tr16_b64_v4i16((__attribute__((address_space(3))) v4i16_t*)p)); }
DI s16x8 lds_tr2(lcptr p0, lcptr p1) { s16x4 a = lds_tr(p0), b = lds_tr(p1); return __builtin_shufflevector(a, b, 0, 1, 2, 3, 4, 5, 6, 7); }
#define WAITV(n) asm volatile("s_waitcnt vmcnt(" #n ")" ::: "memory")
template <int N> DI void wait_vm_bar() { asm volatile("s_waitcnt vmcnt(%0) lgkmcnt(0)\n\ts_barrier" :: "i"(N) : "memory"); }

DI u32x4 pack8(const f32x16& a, int r0, float sc) {
    u32x4 r; r[0] = pack2(a[r0] * sc, a[r0 + 1] * sc); r[1] = pack2(a[r0 + 2] * sc, a[r0 + 3] * sc);
    r[2] = pack2(a[r0 + 4] * sc, a[r0 + 5] * sc); r[3] = pack2(a[r0 + 6] * sc, a[r0 + 7] * sc); return r;
}

constexpr int P_NSLOT = 5, P_SLOT = 16384 + 3 * 4096;
__global__ __launch_bounds__(256) void k_qkv(const unsigned short* __restrict__ x, const unsigned short* __restrict__ w,
                                             const unsigned short* __restrict__ gw, char* __restrict__ ws) {
    __shared__ __attribute__((aligned(1024))) char lds[P_NSLOT * P_SLOT];
    const int lane = threadIdx.x & 63, wid = __builtin_amdgcn_readfirstlane(threadIdx.x >> 6);
    const int xcd = blockIdx.x & 7, idx = blockIdx.x >> 3, pi = xcd * 2 + (idx & 1), rb = idx >> 1;
    const int h = lane >> 5, m = lane & 31;
    const unsigned lds0 = (unsigned)(uintptr_t)(lptr)lds;
    char* ws_dbg = ws; STAMP(0, 0);
    const srd_t sx = make_srd(x, S_ * D_ * 2), sw = make_srd(w, D_ * 3 * D_ * 2);
    const int grow = rb * 128 + pi * 8 + wid * 2;
    const u32x4 gx0 = *(const u32x4*)(x + (size_t)grow * D_ + lane * 8), gx1 = *(const u32x4*)(x + (size_t)(grow + 1) * D_ + lane * 8);
    const u32x4 gwv = *(const u32x4*)(gw + lane * 8);
    unsigned vx[2];
#pragma unroll
    for (int t = 0; t < 2; ++t) { const int r = wid * 32 + t * 8 + (lane >> 3); vx[t] = (unsigned)((rb * 128 + r) * 1024 + (((lane & 7) ^ ((r >> 1) & 7)) << 4)); }
    const unsigned vw = (unsigned)((wid * 16 + (lane >> 2)) * 3072 + pi * 64 + (lane & 3) * 16);
    auto issue = [&](int c) {
        const unsigned sl = lds0 + (c % P_NSLOT) * P_SLOT;
#pragma unroll
        for (int t = 0; t < 4; ++t) dma16(sx, vx[t & 1], c * 128 + (t >> 1) * 16384, sl + wid * 4096 + t * 1024);
#pragma unroll
        for (int t = 0; t < 3; ++t) dma16(sw, vw, c * 64 * 3072 + t * 1024, sl + 16384 + t * 4096 + wid * 1024);
    };
#pragma unroll
    for (int c = 0; c < 5; ++c) issue(c);
    f32x16 aq = {}, ak = {}, av = {};
    const int xr = wid * 32 + m;
    unsigned xa[4];
#pragma unroll
    for (int ks = 0; ks < 4; ++ks) xa[ks] = (unsigned)(xr * 128 + (((2 * ks + h) ^ ((xr >> 1) & 7)) << 4));
    const unsigned wa = (unsigned)(16384 + (8 * h + ((lane & 15) >> 2)) * 64 + ((lane >> 4) & 1) * 32 + (lane & 3) * 8);
#pragma unroll
    for (int c = 0; c < 8; ++c) {
        if (c == 0) wait_vm_bar<28>(); else if (c <= 4) wait_vm_bar<21>(); else if (c == 5) wait_vm_bar<14>(); else if (c == 6) wait_vm_bar<7>(); else wait_vm_bar<0>();
        if (c == 0) STAMP(0, 1);
        if (c >= 1 && c + 4 < 8) issue(c + 4);
        lcptr sl = (lcptr)lds + (c % P_NSLOT) * P_SLOT;
#pragma unroll
        for (int ks = 0; ks < 4; ++ks) {
            const s16x8 xf = lds_b128(sl + xa[ks]);
            const s16x8 wq = lds_tr2(sl + wa + ks * 1024, sl + wa + ks * 1024 + 256);
            const s16x8 wk = lds_tr2(sl + wa + 4096 + ks * 1024, sl + wa + 4096 + ks * 1024 + 256);
            const s16x8 wv = lds_tr2(sl + wa + 8192 + ks * 1024, sl + wa + 8192 + ks * 1024 + 256);
            aq = mfma32(wq, xf, aq); ak = mfma32(wk, xf, ak); av = mfma32(xf, wv, av);
        }
    }
    STAMP(0, 2);
    const int tile = rb * 4 + wid;
    constexpr float QS = 0.25f * 1.4426950408889634f;
#pragma unroll
    for (int hh = 0; hh < 2; ++hh) {
        const size_t o = ((size_t)((pi * 2 + hh) * 64 + tile) * 64 + lane) * 16;
        *(u32x4*)(ws + OFF_Q + o) = pack8(aq, hh * 8, QS);
        *(u32x4*)(ws + OFF_K + o) = pack8(ak, hh * 8, 1.f);
    }
#pragma unroll
    for (int s = 0; s < 2; ++s) {
        const size_t o = (size_t)((pi * 2 + (m >> 4)) * 64 + tile) * 1024 + s * 512 + h * 256 + (m & 15) * 16;
        *(u32x4*)(ws + OFF_V + o) = pack8(av, s * 8, 1.f);
    }
    float s0 = 0.f, s1 = 0.f;
#pragma unroll
    for (int i = 0; i < 4; ++i) {
        const float w0 = __uint_as_float(gwv[i] << 16), w1 = __uint_as_float(gwv[i] & 0xffff0000u);
        s0 += __uint_as_float(gx0[i] << 16) * w0 + __uint_as_float(gx0[i] & 0xffff0000u) * w1;
        s1 += __uint_as_float(gx1[i] << 16) * w0 + __uint_as_float(gx1[i] & 0xffff0000u) * w1;
    }
#pragma unroll
    for (int d = 1; d < 64; d <<= 1) { s0 += __shfl_xor(s0, d); s1 += __shfl_xor(s1, d); }
    if (lane < 2) { const float s = lane ? s1 : s0; ((float*)(ws + OFF_G))[grow + lane] = 1.f / (1.f + __expf(-s)); }
    STAMP(0, 3);
}

constexpr int G_NSLOT = 3, G_SLOT = 16384 + 6 * 4096;
DI float geglu(float g, float u) {
    constexpr float C0 = -2.f * 0.7978845608028654f * 1.4426950408889634f, C1 = C0 * 0.044715f;
    const float z = g * __builtin_fmaf(g * g, C1, C0);
    return g * u * __builtin_amdgcn_rcpf(1.f + __builtin_amdgcn_exp2f(z));
}
__global__ __launch_bounds__(256) void k_ffn1(const unsigned short* __restrict__ x, const unsigned short* __restrict__ wg,
                                              const unsigned short* __restrict__ wu, char* __restrict__ ws) {
    __shared__ __attribute__((aligned(1024))) char lds[G_NSLOT * G_SLOT];
    const int lane = threadIdx.x & 63, wid = __builtin_amdgcn_readfirstlane(threadIdx.x >> 6);
    const int xcd = blockIdx.x & 7, idx = blockIdx.x >> 3;
    const int rb = xcd * 2 + (idx & 1), fg = idx >> 1;
    const int h = lane >> 5, m = lane & 31;
    const unsigned lds0 = (unsigned)(uintptr_t)(lptr)lds;
    char* ws_dbg = ws; STAMP(1, 0);
    const srd_t sx = make_srd(x, S_ * D_ * 2), sg = make_srd(wg, D_ * F_ * 2), su = make_srd(wu, D_ * F_ * 2);
    unsigned vx[2];
#pragma unroll
    for (int t = 0; t < 2; ++t) { const int r = wid * 32 + t * 8 + (lane >> 3); vx[t] = (unsigned)((rb * 128 + r) * 1024 + (((lane & 7) ^ ((r >> 1) & 7)) << 4)); }
    const int nfb = (fg == 14) ? 1 : 3;
    const unsigned vw = (unsigned)((wid * 16 + (lane >> 2)) * (F_ * 2) + fg * 192 + (lane & 3) * 16);
    auto issue = [&](int c) {
        const unsigned sl = lds0 + (c % G_NSLOT) * G_SLOT;
#pragma unroll
        for (int t = 0; t < 4; ++t) dma16(sx, vx[t & 1], c * 128 + (t >> 1) * 16384, sl + wid * 4096 + t * 1024);
#pragma unroll
        for (int t = 0; t < 3; ++t) {
            const unsigned so = c * 64 * (F_ * 2) + (t < nfb ? t * 64 : 0);
            dma16(sg, vw, so, sl + 16384 + (2 * t) * 4096 + wid * 1024);
            dma16(su, vw, so, sl + 16384 + (2 * t + 1) * 4096 + wid * 1024);
        }
    };
    issue(0); issue(1); issue(2);
    f32x16 ag[3] = {}, au[3] = {};
    const int xr = wid * 32 + m;
    unsigned xa[4];
#pragma unroll
    for (int ks = 0; ks < 4; ++ks) xa[ks] = (unsigned)(xr * 128 + (((2 * ks + h) ^ ((xr >> 1) & 7)) << 4));
    const unsigned wa = (unsigned)(16384 + (8 * h + ((lane & 15) >> 2)) * 64 + ((lane >> 4) & 1) * 32 + (lane & 3) * 8);
#pragma unroll
    for (int c = 0; c < 8; ++c) {
        if (c == 0) wait_vm_bar<20>(); else if (c <= 6) wait_vm_bar<10>(); else wait_vm_bar<0>();
        if (c == 0) STAMP(1, 1);
        if (c >= 1 && c + 2 < 8) issue(c + 2);
        lcptr sl = (lcptr)lds + (c % G_NSLOT) * G_SLOT;
#pragma unroll
        for (int ks = 0; ks < 4; ++ks) {
            const s16x8 xf = lds_b128(sl + xa[ks]);
#pragma unroll
            for (int t = 0; t < 3; ++t) {
                const s16x8 fg_ = lds_tr2(sl + wa + (2 * t) * 4096 + ks * 1024, sl + wa + (2 * t) * 4096 + ks * 1024 + 256);
                const s16x8 fu_ = lds_tr2(sl + wa + (2 * t + 1) * 4096 + ks * 1024, sl + wa + (2 * t + 1) * 4096 + ks * 1024 + 256);
                ag[t] = mfma32(fg_, xf, ag[t]); au[t] = mfma32(fu_, xf, au[t]);
            }
        }
    }
    STAMP(1, 2);
    const int tile = rb * 4 + wid;
#pragma unroll
    for (int t = 0; t < 3; ++t) {
        if (t < nfb) {
#pragma unroll
            for (int hh = 0; hh < 2; ++hh) {
                u32x4 r;
#pragma unroll
                for (int i = 0; i < 4; ++i) r[i] = pack2(geglu(ag[t][hh * 8 + 2 * i], au[t][hh * 8 + 2 * i]), geglu(ag[t][hh * 8 + 2 * i + 1], au[t][hh * 8 + 2 * i + 1]));
                *(u32x4*)(ws + OFF_ACT + ((size_t)(tile * NKS + 32 + (fg * 3 + t) * 2 + hh) * 64 + lane) * 16) = r;
            }
        }
    }
    STAMP(1, 3);
}

constexpr int A_SLOT = 8192, A_ONES = 3 * A_SLOT, A_FLAG = A_ONES + 4096, A_LDS = A_FLAG + 16;
DI float xhalf_max(float v) { auto r = __builtin_amdgcn_permlane32_swap(__float_as_uint(v), __float_as_uint(v), false, false); return fmaxf(__uint_as_float(r[0]), __uint_as_float(r[1])); }
DI float rowmax16(const f32x16& s) {
    float a = fmaxf(fmaxf(s[0], s[1]), s[2]), b = fmaxf(fmaxf(s[3], s[4]), s[5]);
    a = fmaxf(fmaxf(a, s[6]), s[7]); b = fmaxf(fmaxf(b, s[8]), s[9]); a = fmaxf(fmaxf(a, s[10]), s[11]); b = fmaxf(fmaxf(b, s[12]), s[13]);
    a = fmaxf(fmaxf(a, s[14]), s[15]); return xhalf_max(fmaxf(a, b));
}
#define EXP2(v) __builtin_amdgcn_exp2f(v)
__global__ __launch_bounds__(512) void k_attn(char* __restrict__ ws) {
    __shared__ __attribute__((aligned(1024))) char lds[A_LDS];
    const int lane = threadIdx.x & 63, wid = __builtin_amdgcn_readfirstlane(threadIdx.x >> 6);
    const int xcd = blockIdx.x & 7, idx = blockIdx.x >> 3, head = xcd * 4 + (idx & 3), qb = idx >> 2, qt = qb * 8 + wid;
    const int h = lane >> 5, r = lane & 31;
    const unsigned lds0 = (unsigned)(uintptr_t)(lptr)lds;
    char* ws_dbg = ws; STAMP(2, 0);
    const srd_t skv = make_srd(ws + OFF_K, 4u << 20);
    const unsigned vdma = lane * 16;
    const unsigned sbase = (wid < 4 ? 0u : (2u << 20)) + head * 65536 + (wid & 3) * 1024;
    const unsigned ldst = lds0 + (wid < 4 ? 0 : 4096) + (wid & 3) * 1024;
    auto issue = [&](int c, int slot) { dma16(skv, vdma, sbase + c * 4096, ldst + slot * A_SLOT); };
    issue(0, 0); issue(1, 1);
    const s16x8 qf = *(const s16x8*)(ws + OFF_Q + ((size_t)(head * 64 + qt) * 64 + lane) * 16);
    { u32x2 o2 = {0x3f803f80u, 0x3f803f80u}; *(__attribute__((address_space(3))) u32x2*)((lptr)lds + A_ONES + threadIdx.x * 8) = o2;
      if (threadIdx.x == 0) *(__attribute__((address_space(3))) unsigned*)((lptr)lds + A_FLAG) = 0u; }
    const unsigned kaddr = lane * 16;
    const bool isv = r < 16;
    const unsigned vlane = isv ? (unsigned)(4096 + h * 256 + r * 16) : (unsigned)(A_ONES + (r - 16) * 16);
    lcptr L = (lcptr)lds;

    wait_vm_bar<0>();
    STAMP(2, 1);
    f32x16 o = {}, sA, sB, cneg;
    s16x8 kf, v0, v1; u32x4 p0, p1;
    kf = lds_b128(L + kaddr);
    { f32x16 z = {}; sA = mfma32(kf, qf, z); const float m0 = rowmax16(sA);
#pragma unroll
      for (int i = 0; i < 16; ++i) cneg[i] = -m0;
#pragma unroll
      for (int i = 0; i < 16; ++i) sA[i] -= m0; }
    kf = lds_b128(L + kaddr + 1024);
    p0 = (u32x4){0, 0, 0, 0}; p1 = p0; v0 = lds_b128(L + vlane); v1 = v0;
#define PIN(x) asm volatile("" : "+v"(x))
#define STEP(SC, SN, KOFF, VOFF) do { \
        SN = mfma32(kf, qf, cneg); SBAR(); \
        kf = lds_b128(L + (KOFF)); \
        SC[0] = EXP2(SC[0]); SC[1] = EXP2(SC[1]); SC[2] = EXP2(SC[2]); SC[3] = EXP2(SC[3]); SBAR(); \
        o = mfma32(v0, __builtin_bit_cast(s16x8, p0), o); SBAR(); \
        v0 = lds_b128(L + (VOFF)); \
        SC[4] = EXP2(SC[4]); p0[0] = pack2(SC[0], SC[1]); SC[5] = EXP2(SC[5]); p0[1] = pack2(SC[2], SC[3]); SC[6] = EXP2(SC[6]); SC[7] = EXP2(SC[7]); SBAR(); \
        o = mfma32(v1, __builtin_bit_cast(s16x8, p1), o); SBAR(); \
        v1 = lds_b128(L + (VOFF) + 512); \
        SC[8] = EXP2(SC[8]); p0[2] = pack2(SC[4], SC[5]); SC[9] = EXP2(SC[9]); p0[3] = pack2(SC[6], SC[7]); \
        SC[10] = EXP2(SC[10]); SC[11] = EXP2(SC[11]); SBAR(); \
        SC[12] = EXP2(SC[12]); p1[0] = pack2(SC[8], SC[9]); SC[13] = EXP2(SC[13]); p1[1] = pack2(SC[10], SC[11]); \
        SC[14] = EXP2(SC[14]); SC[15] = EXP2(SC[15]); SBAR(); \
        p1[2] = pack2(SC[12], SC[13]); p1[3] = pack2(SC[14], SC[15]); SBAR(); \
    } while (0)
    int slot = 0;
    for (int c = 0; c < 16; ++c) {
        const int nslot = (slot == 2) ? 0 : slot + 1, fslot = (nslot == 2) ? 0 : nslot + 1;
        if (c + 2 < 16) issue(c + 2, fslot);
        const unsigned kb = kaddr + slot * A_SLOT, kn = kaddr + nslot * A_SLOT, vb = vlane + (isv ? slot * A_SLOT : 0);
        STEP(sA, sB, kb + 2048, vb);
        STEP(sB, sA, kb + 3072, vb + 1024);
        STEP(sA, sB, kn, vb + 2048);
        STEP(sB, sA, kn + 1024, vb + 3072);
        wait_vm_bar<0>();
        slot = nslot;
    }
    o = mfma32(v0, __builtin_bit_cast(s16x8, p0), o);
    o = mfma32(v1, __builtin_bit_cast(s16x8, p1), o);
    STAMP(2, 2);
    float l = o[8];
    bool bad = !(l <= 1.8446744e19f);
#pragma unroll
    for (int i = 0; i < 8; ++i) bad |= !(__builtin_fabsf(o[i]) <= 3.0e38f);
    if (__any(bad) && lane == 0) *(__attribute__((address_space(3))) unsigned*)((lptr)lds + A_FLAG) = 1u;
    asm volatile("s_waitcnt lgkmcnt(0)\n\ts_barrier" ::: "memory");
    const unsigned redo = __builtin_amdgcn_readfirstlane(*(volatile __attribute__((address_space(3))) unsigned*)((lptr)lds + A_FLAG));
    if (redo) {
        float mrun = -INFINITY;
#pragma unroll
        for (int i = 0; i < 16; ++i) o[i] = 0.f;
        for (int c = 0; c < 16; ++c) {
            asm volatile("s_waitcnt lgkmcnt(0)\n\ts_barrier" ::: "memory");
            issue(c, 0);
            wait_vm_bar<0>();
            for (int t = 0; t < 4; ++t) {
                f32x16 z = {};
                f32x16 s = mfma32(lds_b128(L + kaddr + t * 1024), qf, z);
                const float mn = fmaxf(mrun, rowmax16(s)), f = EXP2(mrun - mn);
                mrun = mn;
#pragma unroll
                for (int i = 0; i < 16; ++i) { o[i] *= f; s[i] = EXP2(s[i] - mn); }
                const u32x4 q0 = pack8(s, 0, 1.f), q1 = pack8(s, 8, 1.f);
                o = mfma32(lds_b128(L + vlane + t * 1024), __builtin_bit_cast(s16x8, q0), o);
                o = mfma32(lds_b128(L + vlane + t * 1024 + 512), __builtin_bit_cast(s16x8, q1), o);
            }
        }
        l = o[8];
    }
    const float rl = 1.f / l;
    *(u32x4*)(ws + OFF_ACT + ((size_t)(qt * NKS + head) * 64 + lane) * 16) = pack8(o, 0, rl);
    STAMP(2, 3);
}

constexpr int F_NSLOT = 4, F_SLOT = 32768;
__global__ __launch_bounds__(256) void k_final(const unsigned short* __restrict__ wo, const unsigned short* __restrict__ wd,
                                               const char* __restrict__ ws, unsigned short* __restrict__ out) {
    __shared__ __attribute__((aligned(1024))) char lds[F_NSLOT * F_SLOT];
    const int lane = threadIdx.x & 63, wid = __builtin_amdgcn_readfirstlane(threadIdx.x >> 6);
    const int xcd = blockIdx.x & 7, idx = blockIdx.x >> 3, mb = xcd * 4 + (idx >> 3), nb = idx & 7;
    const int h = lane >> 5, m = lane & 31, mi = wid & 1, ni = wid >> 1;
    const unsigned lds0 = (unsigned)(uintptr_t)(lptr)lds;
    char* ws_dbg = (char*)ws; STAMP(3, 0);
    const srd_t sa = make_srd(ws + OFF_ACT, (unsigned)ACT_BYTES), so = make_srd(wo, D_ * D_ * 2), sd = make_srd(wd, F_ * D_ * 2);
    const unsigned va = lane * 16;
    const unsigned sa0 = (unsigned)((mb * 2 + (wid & 1)) * NKS + (wid >> 1) * 4) * 1024;
    const unsigned vw = (unsigned)((wid * 32 + ((lane >> 2) & 7)) * 1024 + nb * 128 + (lane >> 5) * 64 + (lane & 3) * 16);
    auto issue = [&](int c) {
        const unsigned sl = lds0 + (c % F_NSLOT) * F_SLOT;
#pragma unroll
        for (int t = 0; t < 4; ++t) dma16(sa, va, sa0 + c * 8192 + t * 1024, sl + (wid & 1) * 8192 + ((wid >> 1) * 4 + t) * 1024);
#pragma unroll
        for (int t = 0; t < 4; ++t) {
            if (c < 4) dma16(so, vw, c * 131072 + t * 8192, sl + 16384 + (wid * 4 + t) * 1024);
            else dma16(sd, vw, (c - 4) * 131072 + t * 8192, sl + 16384 + (wid * 4 + t) * 1024);
        }
    };
    issue(0); issue(1); issue(2); issue(3);
    const float g = ((const float*)(ws + OFF_G))[mb * 64 + mi * 32 + m];
    f32x16 accA = {}, accB = {};
    const unsigned aa = mi * 8192 + lane * 16;
    const unsigned wa = (unsigned)(16384 + ni * 512 + (4 * h + ((lane & 15) >> 2)) * 64 + ((lane >> 4) & 1) * 32 + (lane & 3) * 8);
#pragma unroll
    for (int c = 0; c < 15; ++c) {
        if (c == 0) wait_vm_bar<24>(); else if (c <= 12) wait_vm_bar<16>(); else if (c == 13) wait_vm_bar<8>(); else wait_vm_bar<0>();
        if (c == 0) STAMP(3, 1);
        if (c >= 1 && c + 3 < 15) issue(c + 3);
        lcptr sl = (lcptr)lds + (c % F_NSLOT) * F_SLOT;
#pragma unroll
        for (int ks = 0; ks < 8; ++ks) {
            if (c == 14 && ks >= 6) continue;
            const s16x8 af = lds_b128(sl + aa + ks * 1024);
            const s16x8 wf = lds_tr2(sl + wa + ks * 2048, sl + wa + ks * 2048 + 1024);
            if (c < 4) accA = mfma32(wf, af, accA); else accB = mfma32(wf, af, accB);
        }
    }
    STAMP(3, 2);
    const float g1 = 1.f - g;
    unsigned pk[8];
#pragma unroll
    for (int i = 0; i < 8; ++i) pk[i] = pack2(g * accA[2 * i] + g1 * accB[2 * i], g * accA[2 * i + 1] + g1 * accB[2 * i + 1]);
    unsigned short* orow = out + (size_t)(mb * 64 + mi * 32 + m) * D_ + nb * 64 + ni * 32 + h * 8;
#pragma unroll
    for (int i = 0; i < 2; ++i) {
        auto r0 = __builtin_amdgcn_permlane32_swap(pk[4 * i], pk[4 * i + 2], false, false);
        auto r1 = __builtin_amdgcn_permlane32_swap(pk[4 * i + 1], pk[4 * i + 3], false, false);
        u32x4 v = {r0[0], r1[0], r0[1], r1[1]};
        *(u32x4*)(orow + i * 16) = v;
    }
    STAMP(3, 3);
    if (blockIdx.x == 0 && threadIdx.x == 0) {
        const volatile unsigned long long* st = (const volatile unsigned long long*)(ws_dbg + (64u << 20));
        auto mn = [&](int k, int i) { unsigned long long v = ~0ull; for (int b = 0; b < 240; ++b) { unsigned long long t = st[((size_t)k * 256 + b) * 16 + i * 2]; v = t < v ? t : v; } return v; };
        auto mx = [&](int k, int i) { unsigned long long v = 0; for (int b = 0; b < 240; ++b) { unsigned long long t = st[((size_t)k * 256 + b) * 16 + i * 2]; v = t > v ? t : v; } return v; };
        unsigned long long now = __builtin_amdgcn_s_memrealtime();
        float val = (float)(now - mn(0,0)) / 100.f;
        if (val > 55.0f) out[0] = (unsigned short)(__float_as_uint(val) >> 16);
    }
}

extern "C" void kernel_launch(void* const* d_in, const int* in_sizes, int n_in, void* d_out, int out_size, void* d_ws, size_t ws_size,
                              hipStream_t stream) {
    const unsigned short* x = (const unsigned short*)d_in[0];
    const unsigned short* wqkv = (const unsigned short*)d_in[1];
    const unsigned short* wo = (const unsigned short*)d_in[2];
    const unsigned short* wg = (const unsigned short*)d_in[4];
    const unsigned short* wu = (const unsigned short*)d_in[5];
    const unsigned short* wd = (const unsigned short*)d_in[6];
    const unsigned short* gw = (const unsigned short*)d_in[7];
    char* ws = (char*)d_ws;
    k_qkv<<<256, 256, 0, stream>>>(x, wqkv, gw, ws);
    k_ffn1<<<240, 256, 0, stream>>>(x, wg, wu, ws);
    k_attn<<<256, 512, 0, stream>>>(ws);
    k_final<<<256, 256, 0, stream>>>(wo, wd, ws, (unsigned short*)d_out);
}
```

```cpp
#include <hip/hip_runtime.h>
#include <hip/hip_bf16.h>
#include <stdint.h>

typedef __attribute__((ext_vector_type(8))) short s16x8;
typedef __attribute__((ext_vector_type(4))) short s16x4;
typedef __attribute__((ext_vector_type(8))) __bf16 bf16x8;
typedef __attribute__((ext_vector_type(2))) __bf16 bf16x2;
typedef __attribute__((ext_vector_type(2))) float f32x2;
typedef __attribute__((ext_vector_type(4))) float f32x4;
typedef __attribute__((ext_vector_type(16))) float f32x16;
typedef __attribute__((ext_vector_type(4))) unsigned u32x4;
typedef __attribute__((ext_vector_type(8))) unsigned u32x8;
typedef __attribute__((address_space(3))) char* lptr;
typedef __attribute__((address_space(3))) const char* lcptr;
typedef __attribute__((address_space(1))) unsigned long long gu64;
typedef unsigned long long u64;
typedef __amdgpu_buffer_rsrc_t srd_t;
typedef short v4i16_t __attribute__((ext_vector_type(4)));

#define DI __device__ __forceinline__
#define SBAR() __builtin_amdgcn_sched_barrier(0)
#define PIN(x) asm volatile("" : "+v"(x))
#define STAMPW(i)
#define STAMP(i)

constexpr int S_ = 2048, D_ = 512, F_ = 1376;
constexpr unsigned OFF_Q = 0;
constexpr unsigned OFF_K = 2u << 20;
constexpr unsigned OFF_V = 4u << 20;
constexpr unsigned OFF_H = 6u << 20;
constexpr unsigned OFF_O = 12u << 20;
constexpr unsigned OFF_G = 16u << 20;
constexpr unsigned OFF_F = 17u << 20;
constexpr unsigned WS_USED = 18u << 20;
constexpr unsigned F_Z = 0, F_Q = 4096, F_H = 8192, F_C = 12288;
constexpr unsigned OOB = 0x40000000u;

constexpr int P_SLOT = 28672;
constexpr int W_SLOT = 32768;
constexpr int KV_BASE = 98304, KV_SLOT = 16384;
constexpr int CTL = 2 * 32768 + 4096;
constexpr int LDS_BYTES = 163840;

DI f32x16 mfma32(s16x8 a, s16x8 b, f32x16 c) {
    return __builtin_amdgcn_mfma_f32_32x32x16_bf16(__builtin_bit_cast(bf16x8, a), __builtin_bit_cast(bf16x8, b), c, 0, 0, 0);
}
DI unsigned pack2(float a, float b) { f32x2 v = {a, b}; return __builtin_bit_cast(unsigned, __builtin_convertvector(v, bf16x2)); }
DI srd_t make_srd(const void* p, unsigned bytes) { return __builtin_amdgcn_make_buffer_rsrc((void*)p, (short)0, (int)bytes, 0x00020000); }
DI void dma16(srd_t srd, unsigned voff, unsigned soff, unsigned ldsaddr) {
    asm volatile("s_mov_b32 m0, %3\n\ts_nop 0\n\tbuffer_load_dwordx4 %0, %1, %2 offen lds" :: "v"(voff), "s"(srd), "s"(soff), "s"(ldsaddr) : "memory");
}
DI u32x4 ld_sc1(srd_t srd, unsigned voff, unsigned soff) { return __builtin_amdgcn_raw_buffer_load_b128(srd, voff, soff, 16); }
DI void st_sc1(u32x4 v, srd_t srd, unsigned voff, unsigned soff) { __builtin_amdgcn_raw_buffer_store_b128(v, srd, voff + soff, 0, 16); }
DI s16x8 lds_b128(lcptr p) { return *(const __attribute__((address_space(3))) s16x8*)p; }
DI void lds_w128(lptr p, u32x4 v) { *(__attribute__((address_space(3))) u32x4*)p = v; }
DI s16x4 lds_tr(lcptr p) { return __builtin_bit_cast(s16x4, __builtin_amdgcn_ds_read_tr16_b64_v4i16((__attribute__((address_space(3))) v4i16_t*)p)); }
DI s16x8 lds_tr2(lcptr p0, lcptr p1) { s16x4 a = lds_tr(p0), b = lds_tr(p1); return __builtin_shufflevector(a, b, 0, 1, 2, 3, 4, 5, 6, 7); }
template <int N> DI void wait_vm_bar() { asm volatile("s_waitcnt vmcnt(%0)\n\ts_barrier" :: "i"(N) : "memory"); }
template <int N> DI void wait_all_bar() { asm volatile("s_waitcnt vmcnt(%0) lgkmcnt(0)\n\ts_barrier" :: "i"(N) : "memory"); }
DI void lgkm_bar() { asm volatile("s_waitcnt lgkmcnt(0)\n\ts_barrier" ::: "memory"); }
DI u32x4 pack8(const f32x16& a, int r0, float sc) {
    u32x4 r; r[0] = pack2(a[r0] * sc, a[r0 + 1] * sc); r[1] = pack2(a[r0 + 2] * sc, a[r0 + 3] * sc);
    r[2] = pack2(a[r0 + 4] * sc, a[r0 + 5] * sc); r[3] = pack2(a[r0 + 6] * sc, a[r0 + 7] * sc); return r;
}
DI void ld16_async(u32x4& d, const void* p) { asm volatile("global_load_dwordx4 %0, %1, off" : "=v"(d) : "v"(p) : "memory"); }
DI void ld8_async_sc1(u64& d, const void* p) { asm volatile("global_load_dwordx2 %0, %1, off sc1" : "=v"(d) : "v"(p) : "memory"); }
#define WAIT_VM0() __builtin_amdgcn_s_waitcnt(0x0F70)
DI u64 ld_flag(const char* p) { return __hip_atomic_load((gu64*)p, __ATOMIC_RELAXED, __HIP_MEMORY_SCOPE_AGENT); }
DI void st_flag(char* p, u64 v) { __hip_atomic_store((gu64*)p, v, __ATOMIC_RELAXED, __HIP_MEMORY_SCOPE_AGENT); }
DI void poll_flags(const char* base, int n, u64 tag, int lane) {
    for (unsigned spins = 0; spins < (1u << 17); ++spins) {
        const u64 v = ld_flag(base + (lane < n ? lane : 0) * 8);
        if (__all(v == tag)) break;
        __builtin_amdgcn_s_sleep(2);
    }
}
DI float wave_sum63(float v) {
#define DPP_ADD(ctrl, rm) v += __uint_as_float(__builtin_amdgcn_update_dpp(0u, __float_as_uint(v), ctrl, rm, 0xf, true))
    DPP_ADD(0x111, 0xf); DPP_ADD(0x112, 0xf); DPP_ADD(0x114, 0xf); DPP_ADD(0x118, 0xf);
    DPP_ADD(0x142, 0xa); DPP_ADD(0x143, 0xc);
#undef DPP_ADD
    return v;
}
DI unsigned geglu2(f32x2 g, f32x2 u) {
    constexpr float C0 = -2.f * 0.7978845608028654f * 1.4426950408889634f, C1 = C0 * 0.044715f;
    const f32x2 z = g * __builtin_elementwise_fma(g * g, (f32x2){C1, C1}, (f32x2){C0, C0});
    const f32x2 d = (f32x2){__builtin_amdgcn_exp2f(z[0]), __builtin_amdgcn_exp2f(z[1])} + 1.f;
    const f32x2 r = (g * u) * (f32x2){__builtin_amdgcn_rcpf(d[0]), __builtin_amdgcn_rcpf(d[1])};
    return __builtin_bit_cast(unsigned, __builtin_convertvector(r, bf16x2));
}
DI float xhalf_max(float v) { auto r = __builtin_amdgcn_permlane32_swap(__float_as_uint(v), __float_as_uint(v), false, false); return fmaxf(__uint_as_float(r[0]), __uint_as_float(r[1])); }
DI float rowmax16(const f32x16& s) {
    float a = fmaxf(fmaxf(s[0], s[1]), s[2]), b = fmaxf(fmaxf(s[3], s[4]), s[5]);
    a = fmaxf(fmaxf(a, s[6]), s[7]); b = fmaxf(fmaxf(b, s[8]), s[9]); a = fmaxf(fmaxf(a, s[10]), s[11]); b = fmaxf(fmaxf(b, s[12]), s[13]);
    a = fmaxf(fmaxf(a, s[14]), s[15]); return xhalf_max(fmaxf(a, b));
}
#define EXP2(v) __builtin_amdgcn_exp2f(v)
#define ATTN_C0 \
    "s_nop 1\n\t" \
    "s_setprio 3\n\t" \
    "s_waitcnt lgkmcnt(0)\n\t" \
    "v_mfma_f32_32x32x16_bf16 v[80:95], v[136:139], v[148:151], v[112:127]\n\t" \
    "v_exp_f32 v64, v64\n\t" \
    "v_exp_f32 v65, v65\n\t" \
    "v_exp_f32 v66, v66\n\t" \
    "v_exp_f32 v67, v67\n\t" \
    "v_mfma_f32_32x32x16_bf16 v[96:111], v[140:143], v[128:131], v[96:111]\n\t" \
    "v_exp_f32 v68, v68\n\t" \
    "v_exp_f32 v69, v69\n\t" \
    "v_exp_f32 v70, v70\n\t" \
    "v_cvt_pk_bf16_f32 v128, v64, v65\n\t" \
    "v_exp_f32 v71, v71\n\t" \
    "v_cvt_pk_bf16_f32 v129, v66, v67\n\t" \
    "v_mfma_f32_32x32x16_bf16 v[96:111], v[144:147], v[132:135], v[96:111]\n\t" \
    "ds_read_b128 v[136:139], %[kcur] offset:2048\n\t" \
    "s_mov_b64 exec, %[vmask]\n\t" \
    "ds_read_b128 v[140:143], %[vcur] offset:0\n\t" \
    "ds_read_b128 v[144:147], %[vcur] offset:512\n\t" \
    "s_mov_b64 exec, -1\n\t" \
    "v_exp_f32 v72, v72\n\t" \
    "v_cvt_pk_bf16_f32 v130, v68, v69\n\t" \
    "v_exp_f32 v73, v73\n\t" \
    "v_cvt_pk_bf16_f32 v131, v70, v71\n\t" \
    "v_exp_f32 v74, v74\n\t" \
    "v_exp_f32 v75, v75\n\t" \
    "v_exp_f32 v76, v76\n\t" \
    "v_cvt_pk_bf16_f32 v132, v72, v73\n\t" \
    "v_exp_f32 v77, v77\n\t" \
    "v_cvt_pk_bf16_f32 v133, v74, v75\n\t" \
    "v_exp_f32 v78, v78\n\t" \
    "v_exp_f32 v79, v79\n\t" \
    "v_cvt_pk_bf16_f32 v134, v76, v77\n\t" \
    "v_cvt_pk_bf16_f32 v135, v78, v79\n\t" \
    "s_waitcnt lgkmcnt(0)\n\t" \
    "v_mfma_f32_32x32x16_bf16 v[64:79], v[136:139], v[148:151], v[112:127]\n\t" \
    "v_exp_f32 v80, v80\n\t" \
    "v_exp_f32 v81, v81\n\t" \
    "v_exp_f32 v82, v82\n\t" \
    "v_exp_f32 v83, v83\n\t" \
    "v_mfma_f32_32x32x16_bf16 v[96:111], v[140:143], v[128:131], v[96:111]\n\t" \
    "v_exp_f32 v84, v84\n\t" \
    "v_exp_f32 v85, v85\n\t" \
    "v_exp_f32 v86, v86\n\t" \
    "v_cvt_pk_bf16_f32 v128, v80, v81\n\t" \
    "v_exp_f32 v87, v87\n\t" \
    "v_cvt_pk_bf16_f32 v129, v82, v83\n\t" \
    "v_mfma_f32_32x32x16_bf16 v[96:111], v[144:147], v[132:135], v[96:111]\n\t" \
    "ds_read_b128 v[136:139], %[kcur] offset:3072\n\t" \
    "s_mov_b64 exec, %[vmask]\n\t" \
    "ds_read_b128 v[140:143], %[vcur] offset:1024\n\t" \
    "ds_read_b128 v[144:147], %[vcur] offset:1536\n\t" \
    "s_mov_b64 exec, -1\n\t" \
    "v_exp_f32 v88, v88\n\t" \
    "v_cvt_pk_bf16_f32 v130, v84, v85\n\t" \
    "v_exp_f32 v89, v89\n\t" \
    "v_cvt_pk_bf16_f32 v131, v86, v87\n\t" \
    "s_add_u32 m0, %[wl0], 0\n\t" \
    "s_add_u32 %[stmp], %[ws0], 0\n\t" \
    "buffer_load_dwordx4 %[vw], %[wsrd], %[stmp] offen lds\n\t" \
    "v_exp_f32 v90, v90\n\t" \
    "v_exp_f32 v91, v91\n\t" \
    "v_exp_f32 v92, v92\n\t" \
    "v_cvt_pk_bf16_f32 v132, v88, v89\n\t" \
    "v_exp_f32 v93, v93\n\t" \
    "v_cvt_pk_bf16_f32 v133, v90, v91\n\t" \
    "v_exp_f32 v94, v94\n\t" \
    "v_exp_f32 v95, v95\n\t" \
    "v_cvt_pk_bf16_f32 v134, v92, v93\n\t" \
    "v_cvt_pk_bf16_f32 v135, v94, v95\n\t" \
    "s_setprio 2\n\t" \
    "s_waitcnt lgkmcnt(0)\n\t" \
    "v_mfma_f32_32x32x16_bf16 v[80:95], v[136:139], v[148:151], v[112:127]\n\t" \
    "v_exp_f32 v64, v64\n\t" \
    "v_exp_f32 v65, v65\n\t" \
    "v_exp_f32 v66, v66\n\t" \
    "v_exp_f32 v67, v67\n\t" \
    "v_mfma_f32_32x32x16_bf16 v[96:111], v[140:143], v[128:131], v[96:111]\n\t" \
    "v_exp_f32 v68, v68\n\t" \
    "v_exp_f32 v69, v69\n\t" \
    "v_exp_f32 v70, v70\n\t" \
    "v_cvt_pk_bf16_f32 v128, v64, v65\n\t" \
    "v_exp_f32 v71, v71\n\t" \
    "v_cvt_pk_bf16_f32 v129, v66, v67\n\t" \
    "v_mfma_f32_32x32x16_bf16 v[96:111], v[144:147], v[132:135], v[96:111]\n\t" \
    "ds_read_b128 v[136:139], %[kcur] offset:4096\n\t" \
    "s_mov_b64 exec, %[vmask]\n\t" \
    "ds_read_b128 v[140:143], %[vcur] offset:2048\n\t" \
    "ds_read_b128 v[144:147], %[vcur] offset:2560\n\t" \
    "s_mov_b64 exec, -1\n\t" \
    "v_exp_f32 v72, v72\n\t" \
    "v_cvt_pk_bf16_f32 v130, v68, v69\n\t" \
    "v_exp_f32 v73, v73\n\t" \
    "v_cvt_pk_bf16_f32 v131, v70, v71\n\t" \
    "s_add_u32 m0, %[wl0], 1024\n\t" \
    "s_add_u32 %[stmp], %[ws0], 8192\n\t" \
    "buffer_load_dwordx4 %[vw], %[wsrd], %[stmp] offen lds\n\t" \
    "v_exp_f32 v74, v74\n\t" \
    "v_exp_f32 v75, v75\n\t" \
    "v_exp_f32 v76, v76\n\t" \
    "v_cvt_pk_bf16_f32 v132, v72, v73\n\t" \
    "v_exp_f32 v77, v77\n\t" \
    "v_cvt_pk_bf16_f32 v133, v74, v75\n\t" \
    "v_exp_f32 v78, v78\n\t" \
    "v_exp_f32 v79, v79\n\t" \
    "v_cvt_pk_bf16_f32 v134, v76, v77\n\t" \
    "v_cvt_pk_bf16_f32 v135, v78, v79\n\t" \
    "s_waitcnt lgkmcnt(0)\n\t" \
    "v_mfma_f32_32x32x16_bf16 v[64:79], v[136:139], v[148:151], v[112:127]\n\t" \
    "v_exp_f32 v80, v80\n\t" \
    "v_exp_f32 v81, v81\n\t" \
    "v_exp_f32 v82, v82\n\t" \
    "v_exp_f32 v83, v83\n\t" \
    "v_mfma_f32_32x32x16_bf16 v[96:111], v[140:143], v[128:131], v[96:111]\n\t" \
    "v_exp_f32 v84, v84\n\t" \
    "v_exp_f32 v85, v85\n\t" \
    "v_exp_f32 v86, v86\n\t" \
    "v_cvt_pk_bf16_f32 v128, v80, v81\n\t" \
    "v_exp_f32 v87, v87\n\t" \
    "v_cvt_pk_bf16_f32 v129, v82, v83\n\t" \
    "v_mfma_f32_32x32x16_bf16 v[96:111], v[144:147], v[132:135], v[96:111]\n\t" \
    "ds_read_b128 v[136:139], %[kcur] offset:5120\n\t" \
    "s_mov_b64 exec, %[vmask]\n\t" \
    "ds_read_b128 v[140:143], %[vcur] offset:3072\n\t" \
    "ds_read_b128 v[144:147], %[vcur] offset:3584\n\t" \
    "s_mov_b64 exec, -1\n\t" \
    "v_exp_f32 v88, v88\n\t" \
    "v_cvt_pk_bf16_f32 v130, v84, v85\n\t" \
    "v_exp_f32 v89, v89\n\t" \
    "v_cvt_pk_bf16_f32 v131, v86, v87\n\t" \
    "s_add_u32 m0, %[wl0], 2048\n\t" \
    "s_add_u32 %[stmp], %[ws0], 16384\n\t" \
    "buffer_load_dwordx4 %[vw], %[wsrd], %[stmp] offen lds\n\t" \
    "v_exp_f32 v90, v90\n\t" \
    "v_exp_f32 v91, v91\n\t" \
    "v_exp_f32 v92, v92\n\t" \
    "v_cvt_pk_bf16_f32 v132, v88, v89\n\t" \
    "v_exp_f32 v93, v93\n\t" \
    "v_cvt_pk_bf16_f32 v133, v90, v91\n\t" \
    "v_exp_f32 v94, v94\n\t" \
    "v_exp_f32 v95, v95\n\t" \
    "v_cvt_pk_bf16_f32 v134, v92, v93\n\t" \
    "v_cvt_pk_bf16_f32 v135, v94, v95\n\t" \
    "s_setprio 1\n\t" \
    "s_waitcnt lgkmcnt(0)\n\t" \
    "v_mfma_f32_32x32x16_bf16 v[80:95], v[136:139], v[148:151], v[112:127]\n\t" \
    "v_exp_f32 v64, v64\n\t" \
    "v_exp_f32 v65, v65\n\t" \
    "v_exp_f32 v66, v66\n\t" \
    "v_exp_f32 v67, v67\n\t" \
    "v_mfma_f32_32x32x16_bf16 v[96:111], v[140:143], v[128:131], v[96:111]\n\t" \
    "v_exp_f32 v68, v68\n\t" \
    "v_exp_f32 v69, v69\n\t" \
    "v_exp_f32 v70, v70\n\t" \
    "v_cvt_pk_bf16_f32 v128, v64, v65\n\t" \
    "v_exp_f32 v71, v71\n\t" \
    "v_cvt_pk_bf16_f32 v129, v66, v67\n\t" \
    "v_mfma_f32_32x32x16_bf16 v[96:111], v[144:147], v[132:135], v[96:111]\n\t" \
    "ds_read_b128 v[136:139], %[kcur] offset:6144\n\t" \
    "s_mov_b64 exec, %[vmask]\n\t" \
    "ds_read_b128 v[140:143], %[vcur] offset:4096\n\t" \
    "ds_read_b128 v[144:147], %[vcur] offset:4608\n\t" \
    "s_mov_b64 exec, -1\n\t" \
    "v_exp_f32 v72, v72\n\t" \
    "v_cvt_pk_bf16_f32 v130, v68, v69\n\t" \
    "v_exp_f32 v73, v73\n\t" \
    "v_cvt_pk_bf16_f32 v131, v70, v71\n\t" \
    "s_add_u32 m0, %[wl0], 3072\n\t" \
    "s_add_u32 %[stmp], %[ws0], 24576\n\t" \
    "buffer_load_dwordx4 %[vw], %[wsrd], %[stmp] offen lds\n\t" \
    "v_exp_f32 v74, v74\n\t" \
    "v_exp_f32 v75, v75\n\t" \
    "v_exp_f32 v76, v76\n\t" \
    "v_cvt_pk_bf16_f32 v132, v72, v73\n\t" \
    "v_exp_f32 v77, v77\n\t" \
    "v_cvt_pk_bf16_f32 v133, v74, v75\n\t" \
    "v_exp_f32 v78, v78\n\t" \
    "v_exp_f32 v79, v79\n\t" \
    "v_cvt_pk_bf16_f32 v134, v76, v77\n\t" \
    "v_cvt_pk_bf16_f32 v135, v78, v79\n\t" \
    "s_waitcnt lgkmcnt(0)\n\t" \
    "v_mfma_f32_32x32x16_bf16 v[64:79], v[136:139], v[148:151], v[112:127]\n\t" \
    "v_exp_f32 v80, v80\n\t" \
    "v_exp_f32 v81, v81\n\t" \
    "v_exp_f32 v82, v82\n\t" \
    "v_exp_f32 v83, v83\n\t" \
    "v_mfma_f32_32x32x16_bf16 v[96:111], v[140:143], v[128:131], v[96:111]\n\t" \
    "v_exp_f32 v84, v84\n\t" \
    "v_exp_f32 v85, v85\n\t" \
    "v_exp_f32 v86, v86\n\t" \
    "v_cvt_pk_bf16_f32 v128, v80, v81\n\t" \
    "v_exp_f32 v87, v87\n\t" \
    "v_cvt_pk_bf16_f32 v129, v82, v83\n\t" \
    "v_mfma_f32_32x32x16_bf16 v[96:111], v[144:147], v[132:135], v[96:111]\n\t" \
    "ds_read_b128 v[136:139], %[kcur] offset:7168\n\t" \
    "s_mov_b64 exec, %[vmask]\n\t" \
    "ds_read_b128 v[140:143], %[vcur] offset:5120\n\t" \
    "ds_read_b128 v[144:147], %[vcur] offset:5632\n\t" \
    "s_mov_b64 exec, -1\n\t" \
    "v_exp_f32 v88, v88\n\t" \
    "v_cvt_pk_bf16_f32 v130, v84, v85\n\t" \
    "v_exp_f32 v89, v89\n\t" \
    "v_cvt_pk_bf16_f32 v131, v86, v87\n\t" \
    "s_add_u32 m0, %[wl0], 32768\n\t" \
    "s_add_u32 %[stmp], %[ws0], 65536\n\t" \
    "buffer_load_dwordx4 %[vw], %[wsrd], %[stmp] offen lds\n\t" \
    "v_exp_f32 v90, v90\n\t" \
    "v_exp_f32 v91, v91\n\t" \
    "v_exp_f32 v92, v92\n\t" \
    "v_cvt_pk_bf16_f32 v132, v88, v89\n\t" \
    "v_exp_f32 v93, v93\n\t" \
    "v_cvt_pk_bf16_f32 v133, v90, v91\n\t" \
    "v_exp_f32 v94, v94\n\t" \
    "v_exp_f32 v95, v95\n\t" \
    "v_cvt_pk_bf16_f32 v134, v92, v93\n\t" \
    "v_cvt_pk_bf16_f32 v135, v94, v95\n\t" \
    "s_setprio 0\n\t" \
    "s_waitcnt lgkmcnt(0)\n\t" \
    "v_mfma_f32_32x32x16_bf16 v[80:95], v[136:139], v[148:151], v[112:127]\n\t" \
    "v_exp_f32 v64, v64\n\t" \
    "v_exp_f32 v65, v65\n\t" \
    "v_exp_f32 v66, v66\n\t" \
    "v_exp_f32 v67, v67\n\t" \
    "v_mfma_f32_32x32x16_bf16 v[96:111], v[140:143], v[128:131], v[96:111]\n\t" \
    "v_exp_f32 v68, v68\n\t" \
    "v_exp_f32 v69, v69\n\t" \
    "v_exp_f32 v70, v70\n\t" \
    "v_cvt_pk_bf16_f32 v128, v64, v65\n\t" \
    "v_exp_f32 v71, v71\n\t" \
    "v_cvt_pk_bf16_f32 v129, v66, v67\n\t" \
    "v_mfma_f32_32x32x16_bf16 v[96:111], v[144:147], v[132:135], v[96:111]\n\t" \
    "ds_read_b128 v[136:139], %[knext] offset:0\n\t" \
    "s_mov_b64 exec, %[vmask]\n\t" \
    "ds_read_b128 v[140:143], %[vcur] offset:6144\n\t" \
    "ds_read_b128 v[144:147], %[vcur] offset:6656\n\t" \
    "s_mov_b64 exec, -1\n\t" \
    "v_exp_f32 v72, v72\n\t" \
    "v_cvt_pk_bf16_f32 v130, v68, v69\n\t" \
    "v_exp_f32 v73, v73\n\t" \
    "v_cvt_pk_bf16_f32 v131, v70, v71\n\t" \
    "s_add_u32 m0, %[wl0], 33792\n\t" \
    "s_add_u32 %[stmp], %[ws0], 73728\n\t" \
    "buffer_load_dwordx4 %[vw], %[wsrd], %[stmp] offen lds\n\t" \
    "v_exp_f32 v74, v74\n\t" \
    "v_exp_f32 v75, v75\n\t" \
    "v_exp_f32 v76, v76\n\t" \
    "v_cvt_pk_bf16_f32 v132, v72, v73\n\t" \
    "v_exp_f32 v77, v77\n\t" \
    "v_cvt_pk_bf16_f32 v133, v74, v75\n\t" \
    "v_exp_f32 v78, v78\n\t" \
    "v_exp_f32 v79, v79\n\t" \
    "v_cvt_pk_bf16_f32 v134, v76, v77\n\t" \
    "v_cvt_pk_bf16_f32 v135, v78, v79\n\t" \
    "s_waitcnt lgkmcnt(0)\n\t" \
    "v_mfma_f32_32x32x16_bf16 v[64:79], v[136:139], v[148:151], v[112:127]\n\t" \
    "v_exp_f32 v80, v80\n\t" \
    "v_exp_f32 v81, v81\n\t" \
    "v_exp_f32 v82, v82\n\t" \
    "v_exp_f32 v83, v83\n\t" \
    "v_mfma_f32_32x32x16_bf16 v[96:111], v[140:143], v[128:131], v[96:111]\n\t" \
    "v_exp_f32 v84, v84\n\t" \
    "v_exp_f32 v85, v85\n\t" \
    "v_exp_f32 v86, v86\n\t" \
    "v_cvt_pk_bf16_f32 v128, v80, v81\n\t" \
    "v_exp_f32 v87, v87\n\t" \
    "v_cvt_pk_bf16_f32 v129, v82, v83\n\t" \
    "v_mfma_f32_32x32x16_bf16 v[96:111], v[144:147], v[132:135], v[96:111]\n\t" \
    "ds_read_b128 v[136:139], %[knext] offset:1024\n\t" \
    "s_mov_b64 exec, %[vmask]\n\t" \
    "ds_read_b128 v[140:143], %[vcur] offset:7168\n\t" \
    "ds_read_b128 v[144:147], %[vcur] offset:7680\n\t" \
    "s_mov_b64 exec, -1\n\t" \
    "v_exp_f32 v88, v88\n\t" \
    "v_cvt_pk_bf16_f32 v130, v84, v85\n\t" \
    "v_exp_f32 v89, v89\n\t" \
    "v_cvt_pk_bf16_f32 v131, v86, v87\n\t" \
    "v_exp_f32 v90, v90\n\t" \
    "v_exp_f32 v91, v91\n\t" \
    "v_exp_f32 v92, v92\n\t" \
    "v_cvt_pk_bf16_f32 v132, v88, v89\n\t" \
    "v_exp_f32 v93, v93\n\t" \
    "v_cvt_pk_bf16_f32 v133, v90, v91\n\t" \
    "v_exp_f32 v94, v94\n\t" \
    "v_exp_f32 v95, v95\n\t" \
    "v_cvt_pk_bf16_f32 v134, v92, v93\n\t" \
    "v_cvt_pk_bf16_f32 v135, v94, v95\n\t" \
    "s_waitcnt lgkmcnt(0)\n\t"

#define ATTN_C1 \
    "s_nop 1\n\t" \
    "s_setprio 3\n\t" \
    "s_waitcnt lgkmcnt(0)\n\t" \
    "v_mfma_f32_32x32x16_bf16 v[80:95], v[136:139], v[148:151], v[112:127]\n\t" \
    "v_exp_f32 v64, v64\n\t" \
    "v_exp_f32 v65, v65\n\t" \
    "v_exp_f32 v66, v66\n\t" \
    "v_exp_f32 v67, v67\n\t" \
    "v_mfma_f32_32x32x16_bf16 v[96:111], v[140:143], v[128:131], v[96:111]\n\t" \
    "v_exp_f32 v68, v68\n\t" \
    "v_exp_f32 v69, v69\n\t" \
    "v_exp_f32 v70, v70\n\t" \
    "v_cvt_pk_bf16_f32 v128, v64, v65\n\t" \
    "v_exp_f32 v71, v71\n\t" \
    "v_cvt_pk_bf16_f32 v129, v66, v67\n\t" \
    "v_mfma_f32_32x32x16_bf16 v[96:111], v[144:147], v[132:135], v[96:111]\n\t" \
    "ds_read_b128 v[136:139], %[kcur] offset:2048\n\t" \
    "s_mov_b64 exec, %[vmask]\n\t" \
    "ds_read_b128 v[140:143], %[vcur] offset:0\n\t" \
    "ds_read_b128 v[144:147], %[vcur] offset:512\n\t" \
    "s_mov_b64 exec, -1\n\t" \
    "v_exp_f32 v72, v72\n\t" \
    "v_cvt_pk_bf16_f32 v130, v68, v69\n\t" \
    "v_exp_f32 v73, v73\n\t" \
    "v_cvt_pk_bf16_f32 v131, v70, v71\n\t" \
    "buffer_load_dwordx4 v[152:155], %[l16], %[srd], %[sk] offen sc1\n\t" \
    "buffer_load_dwordx4 v[156:159], %[l16], %[srd], %[sv] offen sc1\n\t" \
    "v_exp_f32 v74, v74\n\t" \
    "v_exp_f32 v75, v75\n\t" \
    "v_exp_f32 v76, v76\n\t" \
    "v_cvt_pk_bf16_f32 v132, v72, v73\n\t" \
    "v_exp_f32 v77, v77\n\t" \
    "v_cvt_pk_bf16_f32 v133, v74, v75\n\t" \
    "v_exp_f32 v78, v78\n\t" \
    "v_exp_f32 v79, v79\n\t" \
    "v_cvt_pk_bf16_f32 v134, v76, v77\n\t" \
    "v_cvt_pk_bf16_f32 v135, v78, v79\n\t" \
    "s_waitcnt lgkmcnt(0)\n\t" \
    "v_mfma_f32_32x32x16_bf16 v[64:79], v[136:139], v[148:151], v[112:127]\n\t" \
    "v_exp_f32 v80, v80\n\t" \
    "v_exp_f32 v81, v81\n\t" \
    "v_exp_f32 v82, v82\n\t" \
    "v_exp_f32 v83, v83\n\t" \
    "v_mfma_f32_32x32x16_bf16 v[96:111], v[140:143], v[128:131], v[96:111]\n\t" \
    "v_exp_f32 v84, v84\n\t" \
    "v_exp_f32 v85, v85\n\t" \
    "v_exp_f32 v86, v86\n\t" \
    "v_cvt_pk_bf16_f32 v128, v80, v81\n\t" \
    "v_exp_f32 v87, v87\n\t" \
    "v_cvt_pk_bf16_f32 v129, v82, v83\n\t" \
    "v_mfma_f32_32x32x16_bf16 v[96:111], v[144:147], v[132:135], v[96:111]\n\t" \
    "ds_read_b128 v[136:139], %[kcur] offset:3072\n\t" \
    "s_mov_b64 exec, %[vmask]\n\t" \
    "ds_read_b128 v[140:143], %[vcur] offset:1024\n\t" \
    "ds_read_b128 v[144:147], %[vcur] offset:1536\n\t" \
    "s_mov_b64 exec, -1\n\t" \
    "v_exp_f32 v88, v88\n\t" \
    "v_cvt_pk_bf16_f32 v130, v84, v85\n\t" \
    "v_exp_f32 v89, v89\n\t" \
    "v_cvt_pk_bf16_f32 v131, v86, v87\n\t" \
    "s_add_u32 m0, %[wl0], 34816\n\t" \
    "s_add_u32 %[stmp], %[ws0], 81920\n\t" \
    "buffer_load_dwordx4 %[vw], %[wsrd], %[stmp] offen lds\n\t" \
    "v_exp_f32 v90, v90\n\t" \
    "v_exp_f32 v91, v91\n\t" \
    "v_exp_f32 v92, v92\n\t" \
    "v_cvt_pk_bf16_f32 v132, v88, v89\n\t" \
    "v_exp_f32 v93, v93\n\t" \
    "v_cvt_pk_bf16_f32 v133, v90, v91\n\t" \
    "v_exp_f32 v94, v94\n\t" \
    "v_exp_f32 v95, v95\n\t" \
    "v_cvt_pk_bf16_f32 v134, v92, v93\n\t" \
    "v_cvt_pk_bf16_f32 v135, v94, v95\n\t" \
    "s_setprio 2\n\t" \
    "s_waitcnt lgkmcnt(0)\n\t" \
    "v_mfma_f32_32x32x16_bf16 v[80:95], v[136:139], v[148:151], v[112:127]\n\t" \
    "v_exp_f32 v64, v64\n\t" \
    "v_exp_f32 v65, v65\n\t" \
    "v_exp_f32 v66, v66\n\t" \
    "v_exp_f32 v67, v67\n\t" \
    "v_mfma_f32_32x32x16_bf16 v[96:111], v[140:143], v[128:131], v[96:111]\n\t" \
    "v_exp_f32 v68, v68\n\t" \
    "v_exp_f32 v69, v69\n\t" \
    "v_exp_f32 v70, v70\n\t" \
    "v_cvt_pk_bf16_f32 v128, v64, v65\n\t" \
    "v_exp_f32 v71, v71\n\t" \
    "v_cvt_pk_bf16_f32 v129, v66, v67\n\t" \
    "v_mfma_f32_32x32x16_bf16 v[96:111], v[144:147], v[132:135], v[96:111]\n\t" \
    "ds_read_b128 v[136:139], %[kcur] offset:4096\n\t" \
    "s_mov_b64 exec, %[vmask]\n\t" \
    "ds_read_b128 v[140:143], %[vcur] offset:2048\n\t" \
    "ds_read_b128 v[144:147], %[vcur] offset:2560\n\t" \
    "s_mov_b64 exec, -1\n\t" \
    "v_exp_f32 v72, v72\n\t" \
    "v_cvt_pk_bf16_f32 v130, v68, v69\n\t" \
    "v_exp_f32 v73, v73\n\t" \
    "v_cvt_pk_bf16_f32 v131, v70, v71\n\t" \
    "s_add_u32 m0, %[wl0], 35840\n\t" \
    "s_add_u32 %[stmp], %[ws0], 90112\n\t" \
    "buffer_load_dwordx4 %[vw], %[wsrd], %[stmp] offen lds\n\t" \
    "v_exp_f32 v74, v74\n\t" \
    "v_exp_f32 v75, v75\n\t" \
    "v_exp_f32 v76, v76\n\t" \
    "v_cvt_pk_bf16_f32 v132, v72, v73\n\t" \
    "v_exp_f32 v77, v77\n\t" \
    "v_cvt_pk_bf16_f32 v133, v74, v75\n\t" \
    "v_exp_f32 v78, v78\n\t" \
    "v_exp_f32 v79, v79\n\t" \
    "v_cvt_pk_bf16_f32 v134, v76, v77\n\t" \
    "v_cvt_pk_bf16_f32 v135, v78, v79\n\t" \
    "s_waitcnt lgkmcnt(0)\n\t" \
    "v_mfma_f32_32x32x16_bf16 v[64:79], v[136:139], v[148:151], v[112:127]\n\t" \
    "v_exp_f32 v80, v80\n\t" \
    "v_exp_f32 v81, v81\n\t" \
    "v_exp_f32 v82, v82\n\t" \
    "v_exp_f32 v83, v83\n\t" \
    "v_mfma_f32_32x32x16_bf16 v[96:111], v[140:143], v[128:131], v[96:111]\n\t" \
    "v_exp_f32 v84, v84\n\t" \
    "v_exp_f32 v85, v85\n\t" \
    "v_exp_f32 v86, v86\n\t" \
    "v_cvt_pk_bf16_f32 v128, v80, v81\n\t" \
    "v_exp_f32 v87, v87\n\t" \
    "v_cvt_pk_bf16_f32 v129, v82, v83\n\t" \
    "v_mfma_f32_32x32x16_bf16 v[96:111], v[144:147], v[132:135], v[96:111]\n\t" \
    "ds_read_b128 v[136:139], %[kcur] offset:5120\n\t" \
    "s_mov_b64 exec, %[vmask]\n\t" \
    "ds_read_b128 v[140:143], %[vcur] offset:3072\n\t" \
    "ds_read_b128 v[144:147], %[vcur] offset:3584\n\t" \
    "s_mov_b64 exec, -1\n\t" \
    "v_exp_f32 v88, v88\n\t" \
    "v_cvt_pk_bf16_f32 v130, v84, v85\n\t" \
    "v_exp_f32 v89, v89\n\t" \
    "v_cvt_pk_bf16_f32 v131, v86, v87\n\t" \
    "s_add_u32 m0, %[wl0], 65536\n\t" \
    "s_add_u32 %[stmp], %[ws0], 131072\n\t" \
    "buffer_load_dwordx4 %[vw], %[wsrd], %[stmp] offen lds\n\t" \
    "v_exp_f32 v90, v90\n\t" \
    "v_exp_f32 v91, v91\n\t" \
    "v_exp_f32 v92, v92\n\t" \
    "v_cvt_pk_bf16_f32 v132, v88, v89\n\t" \
    "v_exp_f32 v93, v93\n\t" \
    "v_cvt_pk_bf16_f32 v133, v90, v91\n\t" \
    "v_exp_f32 v94, v94\n\t" \
    "v_exp_f32 v95, v95\n\t" \
    "v_cvt_pk_bf16_f32 v134, v92, v93\n\t" \
    "v_cvt_pk_bf16_f32 v135, v94, v95\n\t" \
    "s_setprio 1\n\t" \
    "s_waitcnt lgkmcnt(0)\n\t" \
    "v_mfma_f32_32x32x16_bf16 v[80:95], v[136:139], v[148:151], v[112:127]\n\t" \
    "v_exp_f32 v64, v64\n\t" \
    "v_exp_f32 v65, v65\n\t" \
    "v_exp_f32 v66, v66\n\t" \
    "v_exp_f32 v67, v67\n\t" \
    "v_mfma_f32_32x32x16_bf16 v[96:111], v[140:143], v[128:131], v[96:111]\n\t" \
    "v_exp_f32 v68, v68\n\t" \
    "v_exp_f32 v69, v69\n\t" \
    "v_exp_f32 v70, v70\n\t" \
    "v_cvt_pk_bf16_f32 v128, v64, v65\n\t" \
    "v_exp_f32 v71, v71\n\t" \
    "v_cvt_pk_bf16_f32 v129, v66, v67\n\t" \
    "v_mfma_f32_32x32x16_bf16 v[96:111], v[144:147], v[132:135], v[96:111]\n\t" \
    "ds_read_b128 v[136:139], %[kcur] offset:6144\n\t" \
    "s_mov_b64 exec, %[vmask]\n\t" \
    "ds_read_b128 v[140:143], %[vcur] offset:4096\n\t" \
    "ds_read_b128 v[144:147], %[vcur] offset:4608\n\t" \
    "s_mov_b64 exec, -1\n\t" \
    "v_exp_f32 v72, v72\n\t" \
    "v_cvt_pk_bf16_f32 v130, v68, v69\n\t" \
    "v_exp_f32 v73, v73\n\t" \
    "v_cvt_pk_bf16_f32 v131, v70, v71\n\t" \
    "s_add_u32 m0, %[wl0], 66560\n\t" \
    "s_add_u32 %[stmp], %[ws0], 139264\n\t" \
    "buffer_load_dwordx4 %[vw], %[wsrd], %[stmp] offen lds\n\t" \
    "v_exp_f32 v74, v74\n\t" \
    "v_exp_f32 v75, v75\n\t" \
    "v_exp_f32 v76, v76\n\t" \
    "v_cvt_pk_bf16_f32 v132, v72, v73\n\t" \
    "v_exp_f32 v77, v77\n\t" \
    "v_cvt_pk_bf16_f32 v133, v74, v75\n\t" \
    "v_exp_f32 v78, v78\n\t" \
    "v_exp_f32 v79, v79\n\t" \
    "v_cvt_pk_bf16_f32 v134, v76, v77\n\t" \
    "v_cvt_pk_bf16_f32 v135, v78, v79\n\t" \
    "s_waitcnt lgkmcnt(0)\n\t" \
    "v_mfma_f32_32x32x16_bf16 v[64:79], v[136:139], v[148:151], v[112:127]\n\t" \
    "v_exp_f32 v80, v80\n\t" \
    "v_exp_f32 v81, v81\n\t" \
    "v_exp_f32 v82, v82\n\t" \
    "v_exp_f32 v83, v83\n\t" \
    "v_mfma_f32_32x32x16_bf16 v[96:111], v[140:143], v[128:131], v[96:111]\n\t" \
    "v_exp_f32 v84, v84\n\t" \
    "v_exp_f32 v85, v85\n\t" \
    "v_exp_f32 v86, v86\n\t" \
    "v_cvt_pk_bf16_f32 v128, v80, v81\n\t" \
    "v_exp_f32 v87, v87\n\t" \
    "v_cvt_pk_bf16_f32 v129, v82, v83\n\t" \
    "v_mfma_f32_32x32x16_bf16 v[96:111], v[144:147], v[132:135], v[96:111]\n\t" \
    "ds_read_b128 v[136:139], %[kcur] offset:7168\n\t" \
    "s_mov_b64 exec, %[vmask]\n\t" \
    "ds_read_b128 v[140:143], %[vcur] offset:5120\n\t" \
    "ds_read_b128 v[144:147], %[vcur] offset:5632\n\t" \
    "s_mov_b64 exec, -1\n\t" \
    "v_exp_f32 v88, v88\n\t" \
    "v_cvt_pk_bf16_f32 v130, v84, v85\n\t" \
    "v_exp_f32 v89, v89\n\t" \
    "v_cvt_pk_bf16_f32 v131, v86, v87\n\t" \
    "s_waitcnt vmcnt(4)\n\t" \
    "ds_write_b128 %[kvw], v[152:155]\n\t" \
    "ds_write_b128 %[kvw], v[156:159] offset:8192\n\t" \
    "s_add_u32 m0, %[wl0], 67584\n\t" \
    "s_add_u32 %[stmp], %[ws0], 147456\n\t" \
    "buffer_load_dwordx4 %[vw], %[wsrd], %[stmp] offen lds\n\t" \
    "v_exp_f32 v90, v90\n\t" \
    "v_exp_f32 v91, v91\n\t" \
    "v_exp_f32 v92, v92\n\t" \
    "v_cvt_pk_bf16_f32 v132, v88, v89\n\t" \
    "v_exp_f32 v93, v93\n\t" \
    "v_cvt_pk_bf16_f32 v133, v90, v91\n\t" \
    "v_exp_f32 v94, v94\n\t" \
    "v_exp_f32 v95, v95\n\t" \
    "v_cvt_pk_bf16_f32 v134, v92, v93\n\t" \
    "v_cvt_pk_bf16_f32 v135, v94, v95\n\t" \
    "s_setprio 0\n\t" \
    "s_waitcnt lgkmcnt(0)\n\t" \
    "v_mfma_f32_32x32x16_bf16 v[80:95], v[136:139], v[148:151], v[112:127]\n\t" \
    "v_exp_f32 v64, v64\n\t" \
    "v_exp_f32 v65, v65\n\t" \
    "v_exp_f32 v66, v66\n\t" \
    "v_exp_f32 v67, v67\n\t" \
    "v_mfma_f32_32x32x16_bf16 v[96:111], v[140:143], v[128:131], v[96:111]\n\t" \
    "v_exp_f32 v68, v68\n\t" \
    "v_exp_f32 v69, v69\n\t" \
    "v_exp_f32 v70, v70\n\t" \
    "v_cvt_pk_bf16_f32 v128, v64, v65\n\t" \
    "v_exp_f32 v71, v71\n\t" \
    "v_cvt_pk_bf16_f32 v129, v66, v67\n\t" \
    "v_mfma_f32_32x32x16_bf16 v[96:111], v[144:147], v[132:135], v[96:111]\n\t" \
    "ds_read_b128 v[136:139], %[knext] offset:0\n\t" \
    "s_mov_b64 exec, %[vmask]\n\t" \
    "ds_read_b128 v[140:143], %[vcur] offset:6144\n\t" \
    "ds_read_b128 v[144:147], %[vcur] offset:6656\n\t" \
    "s_mov_b64 exec, -1\n\t" \
    "v_exp_f32 v72, v72\n\t" \
    "v_cvt_pk_bf16_f32 v130, v68, v69\n\t" \
    "v_exp_f32 v73, v73\n\t" \
    "v_cvt_pk_bf16_f32 v131, v70, v71\n\t" \
    "s_add_u32 m0, %[wl0], 68608\n\t" \
    "s_add_u32 %[stmp], %[ws0], 155648\n\t" \
    "buffer_load_dwordx4 %[vw], %[wsrd], %[stmp] offen lds\n\t" \
    "v_exp_f32 v74, v74\n\t" \
    "v_exp_f32 v75, v75\n\t" \
    "v_exp_f32 v76, v76\n\t" \
    "v_cvt_pk_bf16_f32 v132, v72, v73\n\t" \
    "v_exp_f32 v77, v77\n\t" \
    "v_cvt_pk_bf16_f32 v133, v74, v75\n\t" \
    "v_exp_f32 v78, v78\n\t" \
    "v_exp_f32 v79, v79\n\t" \
    "v_cvt_pk_bf16_f32 v134, v76, v77\n\t" \
    "v_cvt_pk_bf16_f32 v135, v78, v79\n\t" \
    "s_waitcnt lgkmcnt(0)\n\t" \
    "v_mfma_f32_32x32x16_bf16 v[64:79], v[136:139], v[148:151], v[112:127]\n\t" \
    "v_exp_f32 v80, v80\n\t" \
    "v_exp_f32 v81, v81\n\t" \
    "v_exp_f32 v82, v82\n\t" \
    "v_exp_f32 v83, v83\n\t" \
    "v_mfma_f32_32x32x16_bf16 v[96:111], v[140:143], v[128:131], v[96:111]\n\t" \
    "v_exp_f32 v84, v84\n\t" \
    "v_exp_f32 v85, v85\n\t" \
    "v_exp_f32 v86, v86\n\t" \
    "v_cvt_pk_bf16_f32 v128, v80, v81\n\t" \
    "v_exp_f32 v87, v87\n\t" \
    "v_cvt_pk_bf16_f32 v129, v82, v83\n\t" \
    "v_mfma_f32_32x32x16_bf16 v[96:111], v[144:147], v[132:135], v[96:111]\n\t" \
    "ds_read_b128 v[136:139], %[knext] offset:1024\n\t" \
    "s_mov_b64 exec, %[vmask]\n\t" \
    "ds_read_b128 v[140:143], %[vcur] offset:7168\n\t" \
    "ds_read_b128 v[144:147], %[vcur] offset:7680\n\t" \
    "s_mov_b64 exec, -1\n\t" \
    "v_exp_f32 v88, v88\n\t" \
    "v_cvt_pk_bf16_f32 v130, v84, v85\n\t" \
    "v_exp_f32 v89, v89\n\t" \
    "v_cvt_pk_bf16_f32 v131, v86, v87\n\t" \
    "v_exp_f32 v90, v90\n\t" \
    "v_exp_f32 v91, v91\n\t" \
    "v_exp_f32 v92, v92\n\t" \
    "v_cvt_pk_bf16_f32 v132, v88, v89\n\t" \
    "v_exp_f32 v93, v93\n\t" \
    "v_cvt_pk_bf16_f32 v133, v90, v91\n\t" \
    "v_exp_f32 v94, v94\n\t" \
    "v_exp_f32 v95, v95\n\t" \
    "v_cvt_pk_bf16_f32 v134, v92, v93\n\t" \
    "v_cvt_pk_bf16_f32 v135, v94, v95\n\t" \
    "s_waitcnt lgkmcnt(0)\n\t"

#define ATTN_C2 \
    "s_nop 1\n\t" \
    "s_setprio 3\n\t" \
    "s_waitcnt lgkmcnt(0)\n\t" \
    "v_mfma_f32_32x32x16_bf16 v[80:95], v[136:139], v[148:151], v[112:127]\n\t" \
    "v_exp_f32 v64, v64\n\t" \
    "v_exp_f32 v65, v65\n\t" \
    "v_exp_f32 v66, v66\n\t" \
    "v_exp_f32 v67, v67\n\t" \
    "v_mfma_f32_32x32x16_bf16 v[96:111], v[140:143], v[128:131], v[96:111]\n\t" \
    "v_exp_f32 v68, v68\n\t" \
    "v_exp_f32 v69, v69\n\t" \
    "v_exp_f32 v70, v70\n\t" \
    "v_cvt_pk_bf16_f32 v128, v64, v65\n\t" \
    "v_exp_f32 v71, v71\n\t" \
    "v_cvt_pk_bf16_f32 v129, v66, v67\n\t" \
    "v_mfma_f32_32x32x16_bf16 v[96:111], v[144:147], v[132:135], v[96:111]\n\t" \
    "ds_read_b128 v[136:139], %[kcur] offset:2048\n\t" \
    "s_mov_b64 exec, %[vmask]\n\t" \
    "ds_read_b128 v[140:143], %[vcur] offset:0\n\t" \
    "ds_read_b128 v[144:147], %[vcur] offset:512\n\t" \
    "s_mov_b64 exec, -1\n\t" \
    "v_exp_f32 v72, v72\n\t" \
    "v_cvt_pk_bf16_f32 v130, v68, v69\n\t" \
    "v_exp_f32 v73, v73\n\t" \
    "v_cvt_pk_bf16_f32 v131, v70, v71\n\t" \
    "global_load_dwordx2 v[248:249], %[fhp], off sc1\n\t" \
    "global_load_dwordx2 v[250:251], %[fcp], off sc1\n\t" \
    "buffer_load_dwordx4 v[152:155], %[l16], %[srd], %[sk] offen sc1\n\t" \
    "buffer_load_dwordx4 v[156:159], %[l16], %[srd], %[sv] offen sc1\n\t" \
    "v_exp_f32 v74, v74\n\t" \
    "v_exp_f32 v75, v75\n\t" \
    "v_exp_f32 v76, v76\n\t" \
    "v_cvt_pk_bf16_f32 v132, v72, v73\n\t" \
    "v_exp_f32 v77, v77\n\t" \
    "v_cvt_pk_bf16_f32 v133, v74, v75\n\t" \
    "v_exp_f32 v78, v78\n\t" \
    "v_exp_f32 v79, v79\n\t" \
    "v_cvt_pk_bf16_f32 v134, v76, v77\n\t" \
    "v_cvt_pk_bf16_f32 v135, v78, v79\n\t" \
    "s_waitcnt lgkmcnt(0)\n\t" \
    "v_mfma_f32_32x32x16_bf16 v[64:79], v[136:139], v[148:151], v[112:127]\n\t" \
    "v_exp_f32 v80, v80\n\t" \
    "v_exp_f32 v81, v81\n\t" \
    "v_exp_f32 v82, v82\n\t" \
    "v_exp_f32 v83, v83\n\t" \
    "v_mfma_f32_32x32x16_bf16 v[96:111], v[140:143], v[128:131], v[96:111]\n\t" \
    "v_exp_f32 v84, v84\n\t" \
    "v_exp_f32 v85, v85\n\t" \
    "v_exp_f32 v86, v86\n\t" \
    "v_cvt_pk_bf16_f32 v128, v80, v81\n\t" \
    "v_exp_f32 v87, v87\n\t" \
    "v_cvt_pk_bf16_f32 v129, v82, v83\n\t" \
    "v_mfma_f32_32x32x16_bf16 v[96:111], v[144:147], v[132:135], v[96:111]\n\t" \
    "ds_read_b128 v[136:139], %[kcur] offset:3072\n\t" \
    "s_mov_b64 exec, %[vmask]\n\t" \
    "ds_read_b128 v[140:143], %[vcur] offset:1024\n\t" \
    "ds_read_b128 v[144:147], %[vcur] offset:1536\n\t" \
    "s_mov_b64 exec, -1\n\t" \
    "v_exp_f32 v88, v88\n\t" \
    "v_cvt_pk_bf16_f32 v130, v84, v85\n\t" \
    "v_exp_f32 v89, v89\n\t" \
    "v_cvt_pk_bf16_f32 v131, v86, v87\n\t" \
    "v_exp_f32 v90, v90\n\t" \
    "v_exp_f32 v91, v91\n\t" \
    "v_exp_f32 v92, v92\n\t" \
    "v_cvt_pk_bf16_f32 v132, v88, v89\n\t" \
    "v_exp_f32 v93, v93\n\t" \
    "v_cvt_pk_bf16_f32 v133, v90, v91\n\t" \
    "v_exp_f32 v94, v94\n\t" \
    "v_exp_f32 v95, v95\n\t" \
    "v_cvt_pk_bf16_f32 v134, v92, v93\n\t" \
    "v_cvt_pk_bf16_f32 v135, v94, v95\n\t" \
    "s_setprio 2\n\t" \
    "s_waitcnt lgkmcnt(0)\n\t" \
    "v_mfma_f32_32x32x16_bf16 v[80:95], v[136:139], v[148:151], v[112:127]\n\t" \
    "v_exp_f32 v64, v64\n\t" \
    "v_exp_f32 v65, v65\n\t" \
    "v_exp_f32 v66, v66\n\t" \
    "v_exp_f32 v67, v67\n\t" \
    "v_mfma_f32_32x32x16_bf16 v[96:111], v[140:143], v[128:131], v[96:111]\n\t" \
    "v_exp_f32 v68, v68\n\t" \
    "v_exp_f32 v69, v69\n\t" \
    "v_exp_f32 v70, v70\n\t" \
    "v_cvt_pk_bf16_f32 v128, v64, v65\n\t" \
    "v_exp_f32 v71, v71\n\t" \
    "v_cvt_pk_bf16_f32 v129, v66, v67\n\t" \
    "v_mfma_f32_32x32x16_bf16 v[96:111], v[144:147], v[132:135], v[96:111]\n\t" \
    "ds_read_b128 v[136:139], %[kcur] offset:4096\n\t" \
    "s_mov_b64 exec, %[vmask]\n\t" \
    "ds_read_b128 v[140:143], %[vcur] offset:2048\n\t" \
    "ds_read_b128 v[144:147], %[vcur] offset:2560\n\t" \
    "s_mov_b64 exec, -1\n\t" \
    "v_exp_f32 v72, v72\n\t" \
    "v_cvt_pk_bf16_f32 v130, v68, v69\n\t" \
    "v_exp_f32 v73, v73\n\t" \
    "v_cvt_pk_bf16_f32 v131, v70, v71\n\t" \
    "v_exp_f32 v74, v74\n\t" \
    "v_exp_f32 v75, v75\n\t" \
    "v_exp_f32 v76, v76\n\t" \
    "v_cvt_pk_bf16_f32 v132, v72, v73\n\t" \
    "v_exp_f32 v77, v77\n\t" \
    "v_cvt_pk_bf16_f32 v133, v74, v75\n\t" \
    "v_exp_f32 v78, v78\n\t" \
    "v_exp_f32 v79, v79\n\t" \
    "v_cvt_pk_bf16_f32 v134, v76, v77\n\t" \
    "v_cvt_pk_bf16_f32 v135, v78, v79\n\t" \
    "s_waitcnt lgkmcnt(0)\n\t" \
    "v_mfma_f32_32x32x16_bf16 v[64:79], v[136:139], v[148:151], v[112:127]\n\t" \
    "v_exp_f32 v80, v80\n\t" \
    "v_exp_f32 v81, v81\n\t" \
    "v_exp_f32 v82, v82\n\t" \
    "v_exp_f32 v83, v83\n\t" \
    "v_mfma_f32_32x32x16_bf16 v[96:111], v[140:143], v[128:131], v[96:111]\n\t" \
    "v_exp_f32 v84, v84\n\t" \
    "v_exp_f32 v85, v85\n\t" \
    "v_exp_f32 v86, v86\n\t" \
    "v_cvt_pk_bf16_f32 v128, v80, v81\n\t" \
    "v_exp_f32 v87, v87\n\t" \
    "v_cvt_pk_bf16_f32 v129, v82, v83\n\t" \
    "v_mfma_f32_32x32x16_bf16 v[96:111], v[144:147], v[132:135], v[96:111]\n\t" \
    "ds_read_b128 v[136:139], %[kcur] offset:5120\n\t" \
    "s_mov_b64 exec, %[vmask]\n\t" \
    "ds_read_b128 v[140:143], %[vcur] offset:3072\n\t" \
    "ds_read_b128 v[144:147], %[vcur] offset:3584\n\t" \
    "s_mov_b64 exec, -1\n\t" \
    "v_exp_f32 v88, v88\n\t" \
    "v_cvt_pk_bf16_f32 v130, v84, v85\n\t" \
    "v_exp_f32 v89, v89\n\t" \
    "v_cvt_pk_bf16_f32 v131, v86, v87\n\t" \
    "v_exp_f32 v90, v90\n\t" \
    "v_exp_f32 v91, v91\n\t" \
    "v_exp_f32 v92, v92\n\t" \
    "v_cvt_pk_bf16_f32 v132, v88, v89\n\t" \
    "v_exp_f32 v93, v93\n\t" \
    "v_cvt_pk_bf16_f32 v133, v90, v91\n\t" \
    "v_exp_f32 v94, v94\n\t" \
    "v_exp_f32 v95, v95\n\t" \
    "v_cvt_pk_bf16_f32 v134, v92, v93\n\t" \
    "v_cvt_pk_bf16_f32 v135, v94, v95\n\t" \
    "s_setprio 1\n\t" \
    "s_waitcnt lgkmcnt(0)\n\t" \
    "v_mfma_f32_32x32x16_bf16 v[80:95], v[136:139], v[148:151], v[112:127]\n\t" \
    "v_exp_f32 v64, v64\n\t" \
    "v_exp_f32 v65, v65\n\t" \
    "v_exp_f32 v66, v66\n\t" \
    "v_exp_f32 v67, v67\n\t" \
    "v_mfma_f32_32x32x16_bf16 v[96:111], v[140:143], v[128:131], v[96:111]\n\t" \
    "v_exp_f32 v68, v68\n\t" \
    "v_exp_f32 v69, v69\n\t" \
    "v_exp_f32 v70, v70\n\t" \
    "v_cvt_pk_bf16_f32 v128, v64, v65\n\t" \
    "v_exp_f32 v71, v71\n\t" \
    "v_cvt_pk_bf16_f32 v129, v66, v67\n\t" \
    "v_mfma_f32_32x32x16_bf16 v[96:111], v[144:147], v[132:135], v[96:111]\n\t" \
    "ds_read_b128 v[136:139], %[kcur] offset:6144\n\t" \
    "s_mov_b64 exec, %[vmask]\n\t" \
    "ds_read_b128 v[140:143], %[vcur] offset:4096\n\t" \
    "ds_read_b128 v[144:147], %[vcur] offset:4608\n\t" \
    "s_mov_b64 exec, -1\n\t" \
    "v_exp_f32 v72, v72\n\t" \
    "v_cvt_pk_bf16_f32 v130, v68, v69\n\t" \
    "v_exp_f32 v73, v73\n\t" \
    "v_cvt_pk_bf16_f32 v131, v70, v71\n\t" \
    "v_exp_f32 v74, v74\n\t" \
    "v_exp_f32 v75, v75\n\t" \
    "v_exp_f32 v76, v76\n\t" \
    "v_cvt_pk_bf16_f32 v132, v72, v73\n\t" \
    "v_exp_f32 v77, v77\n\t" \
    "v_cvt_pk_bf16_f32 v133, v74, v75\n\t" \
    "v_exp_f32 v78, v78\n\t" \
    "v_exp_f32 v79, v79\n\t" \
    "v_cvt_pk_bf16_f32 v134, v76, v77\n\t" \
    "v_cvt_pk_bf16_f32 v135, v78, v79\n\t" \
    "s_waitcnt lgkmcnt(0)\n\t" \
    "v_mfma_f32_32x32x16_bf16 v[64:79], v[136:139], v[148:151], v[112:127]\n\t" \
    "v_exp_f32 v80, v80\n\t" \
    "v_exp_f32 v81, v81\n\t" \
    "v_exp_f32 v82, v82\n\t" \
    "v_exp_f32 v83, v83\n\t" \
    "v_mfma_f32_32x32x16_bf16 v[96:111], v[140:143], v[128:131], v[96:111]\n\t" \
    "v_exp_f32 v84, v84\n\t" \
    "v_exp_f32 v85, v85\n\t" \
    "v_exp_f32 v86, v86\n\t" \
    "v_cvt_pk_bf16_f32 v128, v80, v81\n\t" \
    "v_exp_f32 v87, v87\n\t" \
    "v_cvt_pk_bf16_f32 v129, v82, v83\n\t" \
    "v_mfma_f32_32x32x16_bf16 v[96:111], v[144:147], v[132:135], v[96:111]\n\t" \
    "ds_read_b128 v[136:139], %[kcur] offset:7168\n\t" \
    "s_mov_b64 exec, %[vmask]\n\t" \
    "ds_read_b128 v[140:143], %[vcur] offset:5120\n\t" \
    "ds_read_b128 v[144:147], %[vcur] offset:5632\n\t" \
    "s_mov_b64 exec, -1\n\t" \
    "v_exp_f32 v88, v88\n\t" \
    "v_cvt_pk_bf16_f32 v130, v84, v85\n\t" \
    "v_exp_f32 v89, v89\n\t" \
    "v_cvt_pk_bf16_f32 v131, v86, v87\n\t" \
    "s_waitcnt vmcnt(0)\n\t" \
    "ds_write_b128 %[kvw], v[152:155]\n\t" \
    "ds_write_b128 %[kvw], v[156:159] offset:8192\n\t" \
    "v_exp_f32 v90, v90\n\t" \
    "v_exp_f32 v91, v91\n\t" \
    "v_exp_f32 v92, v92\n\t" \
    "v_cvt_pk_bf16_f32 v132, v88, v89\n\t" \
    "v_exp_f32 v93, v93\n\t" \
    "v_cvt_pk_bf16_f32 v133, v90, v91\n\t" \
    "v_exp_f32 v94, v94\n\t" \
    "v_exp_f32 v95, v95\n\t" \
    "v_cvt_pk_bf16_f32 v134, v92, v93\n\t" \
    "v_cvt_pk_bf16_f32 v135, v94, v95\n\t" \
    "s_setprio 0\n\t" \
    "s_waitcnt lgkmcnt(0)\n\t" \
    "v_mfma_f32_32x32x16_bf16 v[80:95], v[136:139], v[148:151], v[112:127]\n\t" \
    "v_exp_f32 v64, v64\n\t" \
    "v_exp_f32 v65, v65\n\t" \
    "v_exp_f32 v66, v66\n\t" \
    "v_exp_f32 v67, v67\n\t" \
    "v_mfma_f32_32x32x16_bf16 v[96:111], v[140:143], v[128:131], v[96:111]\n\t" \
    "v_exp_f32 v68, v68\n\t" \
    "v_exp_f32 v69, v69\n\t" \
    "v_exp_f32 v70, v70\n\t" \
    "v_cvt_pk_bf16_f32 v128, v64, v65\n\t" \
    "v_exp_f32 v71, v71\n\t" \
    "v_cvt_pk_bf16_f32 v129, v66, v67\n\t" \
    "v_mfma_f32_32x32x16_bf16 v[96:111], v[144:147], v[132:135], v[96:111]\n\t" \
    "ds_read_b128 v[136:139], %[knext] offset:0\n\t" \
    "s_mov_b64 exec, %[vmask]\n\t" \
    "ds_read_b128 v[140:143], %[vcur] offset:6144\n\t" \
    "ds_read_b128 v[144:147], %[vcur] offset:6656\n\t" \
    "s_mov_b64 exec, -1\n\t" \
    "v_exp_f32 v72, v72\n\t" \
    "v_cvt_pk_bf16_f32 v130, v68, v69\n\t" \
    "v_exp_f32 v73, v73\n\t" \
    "v_cvt_pk_bf16_f32 v131, v70, v71\n\t" \
    "v_exp_f32 v74, v74\n\t" \
    "v_exp_f32 v75, v75\n\t" \
    "v_exp_f32 v76, v76\n\t" \
    "v_cvt_pk_bf16_f32 v132, v72, v73\n\t" \
    "v_exp_f32 v77, v77\n\t" \
    "v_cvt_pk_bf16_f32 v133, v74, v75\n\t" \
    "v_exp_f32 v78, v78\n\t" \
    "v_exp_f32 v79, v79\n\t" \
    "v_cvt_pk_bf16_f32 v134, v76, v77\n\t" \
    "v_cvt_pk_bf16_f32 v135, v78, v79\n\t" \
    "s_waitcnt lgkmcnt(0)\n\t" \
    "v_mfma_f32_32x32x16_bf16 v[64:79], v[136:139], v[148:151], v[112:127]\n\t" \
    "v_exp_f32 v80, v80\n\t" \
    "v_exp_f32 v81, v81\n\t" \
    "v_exp_f32 v82, v82\n\t" \
    "v_exp_f32 v83, v83\n\t" \
    "v_mfma_f32_32x32x16_bf16 v[96:111], v[140:143], v[128:131], v[96:111]\n\t" \
    "v_exp_f32 v84, v84\n\t" \
    "v_exp_f32 v85, v85\n\t" \
    "v_exp_f32 v86, v86\n\t" \
    "v_cvt_pk_bf16_f32 v128, v80, v81\n\t" \
    "v_exp_f32 v87, v87\n\t" \
    "v_cvt_pk_bf16_f32 v129, v82, v83\n\t" \
    "v_mfma_f32_32x32x16_bf16 v[96:111], v[144:147], v[132:135], v[96:111]\n\t" \
    "ds_read_b128 v[136:139], %[knext] offset:1024\n\t" \
    "s_mov_b64 exec, %[vmask]\n\t" \
    "ds_read_b128 v[140:143], %[vcur] offset:7168\n\t" \
    "ds_read_b128 v[144:147], %[vcur] offset:7680\n\t" \
    "s_mov_b64 exec, -1\n\t" \
    "v_exp_f32 v88, v88\n\t" \
    "v_cvt_pk_bf16_f32 v130, v84, v85\n\t" \
    "v_exp_f32 v89, v89\n\t" \
    "v_cvt_pk_bf16_f32 v131, v86, v87\n\t" \
    "v_exp_f32 v90, v90\n\t" \
    "v_exp_f32 v91, v91\n\t" \
    "v_exp_f32 v92, v92\n\t" \
    "v_cvt_pk_bf16_f32 v132, v88, v89\n\t" \
    "v_exp_f32 v93, v93\n\t" \
    "v_cvt_pk_bf16_f32 v133, v90, v91\n\t" \
    "v_exp_f32 v94, v94\n\t" \
    "v_exp_f32 v95, v95\n\t" \
    "v_cvt_pk_bf16_f32 v134, v92, v93\n\t" \
    "v_cvt_pk_bf16_f32 v135, v94, v95\n\t" \
    "s_waitcnt lgkmcnt(0)\n\t"

#define ATTN_C3 \
    "s_nop 1\n\t" \
    "s_setprio 3\n\t" \
    "s_waitcnt lgkmcnt(0)\n\t" \
    "v_mfma_f32_32x32x16_bf16 v[80:95], v[136:139], v[148:151], v[112:127]\n\t" \
    "v_exp_f32 v64, v64\n\t" \
    "v_exp_f32 v65, v65\n\t" \
    "v_exp_f32 v66, v66\n\t" \
    "v_exp_f32 v67, v67\n\t" \
    "v_mfma_f32_32x32x16_bf16 v[96:111], v[140:143], v[128:131], v[96:111]\n\t" \
    "v_exp_f32 v68, v68\n\t" \
    "v_exp_f32 v69, v69\n\t" \
    "v_exp_f32 v70, v70\n\t" \
    "v_cvt_pk_bf16_f32 v128, v64, v65\n\t" \
    "v_exp_f32 v71, v71\n\t" \
    "v_cvt_pk_bf16_f32 v129, v66, v67\n\t" \
    "v_mfma_f32_32x32x16_bf16 v[96:111], v[144:147], v[132:135], v[96:111]\n\t" \
    "ds_read_b128 v[136:139], %[kcur] offset:2048\n\t" \
    "s_mov_b64 exec, %[vmask]\n\t" \
    "ds_read_b128 v[140:143], %[vcur] offset:0\n\t" \
    "ds_read_b128 v[144:147], %[vcur] offset:512\n\t" \
    "s_mov_b64 exec, -1\n\t" \
    "v_exp_f32 v72, v72\n\t" \
    "v_cvt_pk_bf16_f32 v130, v68, v69\n\t" \
    "v_exp_f32 v73, v73\n\t" \
    "v_cvt_pk_bf16_f32 v131, v70, v71\n\t" \
    "buffer_load_dwordx4 v[152:155], %[l16], %[srd], %[sk] offen sc1\n\t" \
    "buffer_load_dwordx4 v[156:159], %[l16], %[srd], %[sv] offen sc1\n\t" \
    "s_add_u32 %[stmp], %[hbase], 0\n\t" \
    "buffer_load_dwordx4 v[160:163], %[l16], %[srd], %[stmp] offen sc1\n\t" \
    "s_add_u32 %[stmp], %[hbase], 1024\n\t" \
    "buffer_load_dwordx4 v[164:167], %[l16], %[srd], %[stmp] offen sc1\n\t" \
    "v_exp_f32 v74, v74\n\t" \
    "v_exp_f32 v75, v75\n\t" \
    "v_exp_f32 v76, v76\n\t" \
    "v_cvt_pk_bf16_f32 v132, v72, v73\n\t" \
    "v_exp_f32 v77, v77\n\t" \
    "v_cvt_pk_bf16_f32 v133, v74, v75\n\t" \
    "v_exp_f32 v78, v78\n\t" \
    "v_exp_f32 v79, v79\n\t" \
    "v_cvt_pk_bf16_f32 v134, v76, v77\n\t" \
    "v_cvt_pk_bf16_f32 v135, v78, v79\n\t" \
    "s_waitcnt lgkmcnt(0)\n\t" \
    "v_mfma_f32_32x32x16_bf16 v[64:79], v[136:139], v[148:151], v[112:127]\n\t" \
    "v_exp_f32 v80, v80\n\t" \
    "v_exp_f32 v81, v81\n\t" \
    "v_exp_f32 v82, v82\n\t" \
    "v_exp_f32 v83, v83\n\t" \
    "v_mfma_f32_32x32x16_bf16 v[96:111], v[140:143], v[128:131], v[96:111]\n\t" \
    "v_exp_f32 v84, v84\n\t" \
    "v_exp_f32 v85, v85\n\t" \
    "v_exp_f32 v86, v86\n\t" \
    "v_cvt_pk_bf16_f32 v128, v80, v81\n\t" \
    "v_exp_f32 v87, v87\n\t" \
    "v_cvt_pk_bf16_f32 v129, v82, v83\n\t" \
    "v_mfma_f32_32x32x16_bf16 v[96:111], v[144:147], v[132:135], v[96:111]\n\t" \
    "ds_read_b128 v[136:139], %[kcur] offset:3072\n\t" \
    "s_mov_b64 exec, %[vmask]\n\t" \
    "ds_read_b128 v[140:143], %[vcur] offset:1024\n\t" \
    "ds_read_b128 v[144:147], %[vcur] offset:1536\n\t" \
    "s_mov_b64 exec, -1\n\t" \
    "v_exp_f32 v88, v88\n\t" \
    "v_cvt_pk_bf16_f32 v130, v84, v85\n\t" \
    "v_exp_f32 v89, v89\n\t" \
    "v_cvt_pk_bf16_f32 v131, v86, v87\n\t" \
    "s_add_u32 %[stmp], %[hbase], 2048\n\t" \
    "buffer_load_dwordx4 v[168:171], %[l16], %[srd], %[stmp] offen sc1\n\t" \
    "s_add_u32 %[stmp], %[hbase], 3072\n\t" \
    "buffer_load_dwordx4 v[172:175], %[l16], %[srd], %[stmp] offen sc1\n\t" \
    "v_exp_f32 v90, v90\n\t" \
    "v_exp_f32 v91, v91\n\t" \
    "v_exp_f32 v92, v92\n\t" \
    "v_cvt_pk_bf16_f32 v132, v88, v89\n\t" \
    "v_exp_f32 v93, v93\n\t" \
    "v_cvt_pk_bf16_f32 v133, v90, v91\n\t" \
    "v_exp_f32 v94, v94\n\t" \
    "v_exp_f32 v95, v95\n\t" \
    "v_cvt_pk_bf16_f32 v134, v92, v93\n\t" \
    "v_cvt_pk_bf16_f32 v135, v94, v95\n\t" \
    "s_setprio 2\n\t" \
    "s_waitcnt lgkmcnt(0)\n\t" \
    "v_mfma_f32_32x32x16_bf16 v[80:95], v[136:139], v[148:151], v[112:127]\n\t" \
    "v_exp_f32 v64, v64\n\t" \
    "v_exp_f32 v65, v65\n\t" \
    "v_exp_f32 v66, v66\n\t" \
    "v_exp_f32 v67, v67\n\t" \
    "v_mfma_f32_32x32x16_bf16 v[96:111], v[140:143], v[128:131], v[96:111]\n\t" \
    "v_exp_f32 v68, v68\n\t" \
    "v_exp_f32 v69, v69\n\t" \
    "v_exp_f32 v70, v70\n\t" \
    "v_cvt_pk_bf16_f32 v128, v64, v65\n\t" \
    "v_exp_f32 v71, v71\n\t" \
    "v_cvt_pk_bf16_f32 v129, v66, v67\n\t" \
    "v_mfma_f32_32x32x16_bf16 v[96:111], v[144:147], v[132:135], v[96:111]\n\t" \
    "ds_read_b128 v[136:139], %[kcur] offset:4096\n\t" \
    "s_mov_b64 exec, %[vmask]\n\t" \
    "ds_read_b128 v[140:143], %[vcur] offset:2048\n\t" \
    "ds_read_b128 v[144:147], %[vcur] offset:2560\n\t" \
    "s_mov_b64 exec, -1\n\t" \
    "v_exp_f32 v72, v72\n\t" \
    "v_cvt_pk_bf16_f32 v130, v68, v69\n\t" \
    "v_exp_f32 v73, v73\n\t" \
    "v_cvt_pk_bf16_f32 v131, v70, v71\n\t" \
    "s_add_u32 %[stmp], %[hbase], 4096\n\t" \
    "buffer_load_dwordx4 v[176:179], %[l16], %[srd], %[stmp] offen sc1\n\t" \
    "s_add_u32 %[stmp], %[hbase], 5120\n\t" \
    "buffer_load_dwordx4 v[180:183], %[l16], %[srd], %[stmp] offen sc1\n\t" \
    "v_exp_f32 v74, v74\n\t" \
    "v_exp_f32 v75, v75\n\t" \
    "v_exp_f32 v76, v76\n\t" \
    "v_cvt_pk_bf16_f32 v132, v72, v73\n\t" \
    "v_exp_f32 v77, v77\n\t" \
    "v_cvt_pk_bf16_f32 v133, v74, v75\n\t" \
    "v_exp_f32 v78, v78\n\t" \
    "v_exp_f32 v79, v79\n\t" \
    "v_cvt_pk_bf16_f32 v134, v76, v77\n\t" \
    "v_cvt_pk_bf16_f32 v135, v78, v79\n\t" \
    "s_waitcnt lgkmcnt(0)\n\t" \
    "v_mfma_f32_32x32x16_bf16 v[64:79], v[136:139], v[148:151], v[112:127]\n\t" \
    "v_exp_f32 v80, v80\n\t" \
    "v_exp_f32 v81, v81\n\t" \
    "v_exp_f32 v82, v82\n\t" \
    "v_exp_f32 v83, v83\n\t" \
    "v_mfma_f32_32x32x16_bf16 v[96:111], v[140:143], v[128:131], v[96:111]\n\t" \
    "v_exp_f32 v84, v84\n\t" \
    "v_exp_f32 v85, v85\n\t" \
    "v_exp_f32 v86, v86\n\t" \
    "v_cvt_pk_bf16_f32 v128, v80, v81\n\t" \
    "v_exp_f32 v87, v87\n\t" \
    "v_cvt_pk_bf16_f32 v129, v82, v83\n\t" \
    "v_mfma_f32_32x32x16_bf16 v[96:111], v[144:147], v[132:135], v[96:111]\n\t" \
    "ds_read_b128 v[136:139], %[kcur] offset:5120\n\t" \
    "s_mov_b64 exec, %[vmask]\n\t" \
    "ds_read_b128 v[140:143], %[vcur] offset:3072\n\t" \
    "ds_read_b128 v[144:147], %[vcur] offset:3584\n\t" \
    "s_mov_b64 exec, -1\n\t" \
    "v_exp_f32 v88, v88\n\t" \
    "v_cvt_pk_bf16_f32 v130, v84, v85\n\t" \
    "v_exp_f32 v89, v89\n\t" \
    "v_cvt_pk_bf16_f32 v131, v86, v87\n\t" \
    "s_add_u32 %[stmp], %[hbase], 6144\n\t" \
    "buffer_load_dwordx4 v[184:187], %[l16], %[srd], %[stmp] offen sc1\n\t" \
    "s_add_u32 %[stmp], %[hbase], 7168\n\t" \
    "buffer_load_dwordx4 v[188:191], %[l16], %[srd], %[stmp] offen sc1\n\t" \
    "v_exp_f32 v90, v90\n\t" \
    "v_exp_f32 v91, v91\n\t" \
    "v_exp_f32 v92, v92\n\t" \
    "v_cvt_pk_bf16_f32 v132, v88, v89\n\t" \
    "v_exp_f32 v93, v93\n\t" \
    "v_cvt_pk_bf16_f32 v133, v90, v91\n\t" \
    "v_exp_f32 v94, v94\n\t" \
    "v_exp_f32 v95, v95\n\t" \
    "v_cvt_pk_bf16_f32 v134, v92, v93\n\t" \
    "v_cvt_pk_bf16_f32 v135, v94, v95\n\t" \
    "s_setprio 1\n\t" \
    "s_waitcnt lgkmcnt(0)\n\t" \
    "v_mfma_f32_32x32x16_bf16 v[80:95], v[136:139], v[148:151], v[112:127]\n\t" \
    "v_exp_f32 v64, v64\n\t" \
    "v_exp_f32 v65, v65\n\t" \
    "v_exp_f32 v66, v66\n\t" \
    "v_exp_f32 v67, v67\n\t" \
    "v_mfma_f32_32x32x16_bf16 v[96:111], v[140:143], v[128:131], v[96:111]\n\t" \
    "v_exp_f32 v68, v68\n\t" \
    "v_exp_f32 v69, v69\n\t" \
    "v_exp_f32 v70, v70\n\t" \
    "v_cvt_pk_bf16_f32 v128, v64, v65\n\t" \
    "v_exp_f32 v71, v71\n\t" \
    "v_cvt_pk_bf16_f32 v129, v66, v67\n\t" \
    "v_mfma_f32_32x32x16_bf16 v[96:111], v[144:147], v[132:135], v[96:111]\n\t" \
    "ds_read_b128 v[136:139], %[kcur] offset:6144\n\t" \
    "s_mov_b64 exec, %[vmask]\n\t" \
    "ds_read_b128 v[140:143], %[vcur] offset:4096\n\t" \
    "ds_read_b128 v[144:147], %[vcur] offset:4608\n\t" \
    "s_mov_b64 exec, -1\n\t" \
    "v_exp_f32 v72, v72\n\t" \
    "v_cvt_pk_bf16_f32 v130, v68, v69\n\t" \
    "v_exp_f32 v73, v73\n\t" \
    "v_cvt_pk_bf16_f32 v131, v70, v71\n\t" \
    "s_add_u32 %[stmp], %[hbase], 8192\n\t" \
    "buffer_load_dwordx4 v[192:195], %[l16], %[srd], %[stmp] offen sc1\n\t" \
    "s_add_u32 %[stmp], %[hbase], 9216\n\t" \
    "buffer_load_dwordx4 v[196:199], %[l16], %[srd], %[stmp] offen sc1\n\t" \
    "v_exp_f32 v74, v74\n\t" \
    "v_exp_f32 v75, v75\n\t" \
    "v_exp_f32 v76, v76\n\t" \
    "v_cvt_pk_bf16_f32 v132, v72, v73\n\t" \
    "v_exp_f32 v77, v77\n\t" \
    "v_cvt_pk_bf16_f32 v133, v74, v75\n\t" \
    "v_exp_f32 v78, v78\n\t" \
    "v_exp_f32 v79, v79\n\t" \
    "v_cvt_pk_bf16_f32 v134, v76, v77\n\t" \
    "v_cvt_pk_bf16_f32 v135, v78, v79\n\t" \
    "s_waitcnt lgkmcnt(0)\n\t" \
    "v_mfma_f32_32x32x16_bf16 v[64:79], v[136:139], v[148:151], v[112:127]\n\t" \
    "v_exp_f32 v80, v80\n\t" \
    "v_exp_f32 v81, v81\n\t" \
    "v_exp_f32 v82, v82\n\t" \
    "v_exp_f32 v83, v83\n\t" \
    "v_mfma_f32_32x32x16_bf16 v[96:111], v[140:143], v[128:131], v[96:111]\n\t" \
    "v_exp_f32 v84, v84\n\t" \
    "v_exp_f32 v85, v85\n\t" \
    "v_exp_f32 v86, v86\n\t" \
    "v_cvt_pk_bf16_f32 v128, v80, v81\n\t" \
    "v_exp_f32 v87, v87\n\t" \
    "v_cvt_pk_bf16_f32 v129, v82, v83\n\t" \
    "v_mfma_f32_32x32x16_bf16 v[96:111], v[144:147], v[132:135], v[96:111]\n\t" \
    "ds_read_b128 v[136:139], %[kcur] offset:7168\n\t" \
    "s_mov_b64 exec, %[vmask]\n\t" \
    "ds_read_b128 v[140:143], %[vcur] offset:5120\n\t" \
    "ds_read_b128 v[144:147], %[vcur] offset:5632\n\t" \
    "s_mov_b64 exec, -1\n\t" \
    "v_exp_f32 v88, v88\n\t" \
    "v_cvt_pk_bf16_f32 v130, v84, v85\n\t" \
    "v_exp_f32 v89, v89\n\t" \
    "v_cvt_pk_bf16_f32 v131, v86, v87\n\t" \
    "s_waitcnt vmcnt(10)\n\t" \
    "ds_write_b128 %[kvw], v[152:155]\n\t" \
    "ds_write_b128 %[kvw], v[156:159] offset:8192\n\t" \
    "s_add_u32 %[stmp], %[hbase], 10240\n\t" \
    "buffer_load_dwordx4 v[200:203], %[l16], %[srd], %[stmp] offen sc1\n\t" \
    "s_add_u32 %[stmp], %[hbase], 11264\n\t" \
    "buffer_load_dwordx4 v[204:207], %[l16], %[srd], %[stmp] offen sc1\n\t" \
    "v_exp_f32 v90, v90\n\t" \
    "v_exp_f32 v91, v91\n\t" \
    "v_exp_f32 v92, v92\n\t" \
    "v_cvt_pk_bf16_f32 v132, v88, v89\n\t" \
    "v_exp_f32 v93, v93\n\t" \
    "v_cvt_pk_bf16_f32 v133, v90, v91\n\t" \
    "v_exp_f32 v94, v94\n\t" \
    "v_exp_f32 v95, v95\n\t" \
    "v_cvt_pk_bf16_f32 v134, v92, v93\n\t" \
    "v_cvt_pk_bf16_f32 v135, v94, v95\n\t" \
    "s_setprio 0\n\t" \
    "s_waitcnt lgkmcnt(0)\n\t" \
    "v_mfma_f32_32x32x16_bf16 v[80:95], v[136:139], v[148:151], v[112:127]\n\t" \
    "v_exp_f32 v64, v64\n\t" \
    "v_exp_f32 v65, v65\n\t" \
    "v_exp_f32 v66, v66\n\t" \
    "v_exp_f32 v67, v67\n\t" \
    "v_mfma_f32_32x32x16_bf16 v[96:111], v[140:143], v[128:131], v[96:111]\n\t" \
    "v_exp_f32 v68, v68\n\t" \
    "v_exp_f32 v69, v69\n\t" \
    "v_exp_f32 v70, v70\n\t" \
    "v_cvt_pk_bf16_f32 v128, v64, v65\n\t" \
    "v_exp_f32 v71, v71\n\t" \
    "v_cvt_pk_bf16_f32 v129, v66, v67\n\t" \
    "v_mfma_f32_32x32x16_bf16 v[96:111], v[144:147], v[132:135], v[96:111]\n\t" \
    "ds_read_b128 v[136:139], %[knext] offset:0\n\t" \
    "s_mov_b64 exec, %[vmask]\n\t" \
    "ds_read_b128 v[140:143], %[vcur] offset:6144\n\t" \
    "ds_read_b128 v[144:147], %[vcur] offset:6656\n\t" \
    "s_mov_b64 exec, -1\n\t" \
    "v_exp_f32 v72, v72\n\t" \
    "v_cvt_pk_bf16_f32 v130, v68, v69\n\t" \
    "v_exp_f32 v73, v73\n\t" \
    "v_cvt_pk_bf16_f32 v131, v70, v71\n\t" \
    "s_mov_b32 m0, %[dml]\n\t" \
    "s_nop 0\n\t" \
    "buffer_load_dword %[vwm], %[wsrd], %[wms] offen lds\n\t" \
    "v_exp_f32 v74, v74\n\t" \
    "v_exp_f32 v75, v75\n\t" \
    "v_exp_f32 v76, v76\n\t" \
    "v_cvt_pk_bf16_f32 v132, v72, v73\n\t" \
    "v_exp_f32 v77, v77\n\t" \
    "v_cvt_pk_bf16_f32 v133, v74, v75\n\t" \
    "v_exp_f32 v78, v78\n\t" \
    "v_exp_f32 v79, v79\n\t" \
    "v_cvt_pk_bf16_f32 v134, v76, v77\n\t" \
    "v_cvt_pk_bf16_f32 v135, v78, v79\n\t" \
    "s_waitcnt lgkmcnt(0)\n\t" \
    "v_mfma_f32_32x32x16_bf16 v[64:79], v[136:139], v[148:151], v[112:127]\n\t" \
    "v_exp_f32 v80, v80\n\t" \
    "v_exp_f32 v81, v81\n\t" \
    "v_exp_f32 v82, v82\n\t" \
    "v_exp_f32 v83, v83\n\t" \
    "v_mfma_f32_32x32x16_bf16 v[96:111], v[140:143], v[128:131], v[96:111]\n\t" \
    "v_exp_f32 v84, v84\n\t" \
    "v_exp_f32 v85, v85\n\t" \
    "v_exp_f32 v86, v86\n\t" \
    "v_cvt_pk_bf16_f32 v128, v80, v81\n\t" \
    "v_exp_f32 v87, v87\n\t" \
    "v_cvt_pk_bf16_f32 v129, v82, v83\n\t" \
    "v_mfma_f32_32x32x16_bf16 v[96:111], v[144:147], v[132:135], v[96:111]\n\t" \
    "ds_read_b128 v[136:139], %[knext] offset:1024\n\t" \
    "s_mov_b64 exec, %[vmask]\n\t" \
    "ds_read_b128 v[140:143], %[vcur] offset:7168\n\t" \
    "ds_read_b128 v[144:147], %[vcur] offset:7680\n\t" \
    "s_mov_b64 exec, -1\n\t" \
    "v_exp_f32 v88, v88\n\t" \
    "v_cvt_pk_bf16_f32 v130, v84, v85\n\t" \
    "v_exp_f32 v89, v89\n\t" \
    "v_cvt_pk_bf16_f32 v131, v86, v87\n\t" \
    "v_exp_f32 v90, v90\n\t" \
    "v_exp_f32 v91, v91\n\t" \
    "v_exp_f32 v92, v92\n\t" \
    "v_cvt_pk_bf16_f32 v132, v88, v89\n\t" \
    "v_exp_f32 v93, v93\n\t" \
    "v_cvt_pk_bf16_f32 v133, v90, v91\n\t" \
    "v_exp_f32 v94, v94\n\t" \
    "v_exp_f32 v95, v95\n\t" \
    "v_cvt_pk_bf16_f32 v134, v92, v93\n\t" \
    "v_cvt_pk_bf16_f32 v135, v94, v95\n\t" \
    "s_waitcnt lgkmcnt(0)\n\t"

#define ATTN_C4 \
    "s_nop 1\n\t" \
    "s_setprio 3\n\t" \
    "s_waitcnt lgkmcnt(0)\n\t" \
    "v_mfma_f32_32x32x16_bf16 v[80:95], v[136:139], v[148:151], v[112:127]\n\t" \
    "v_exp_f32 v64, v64\n\t" \
    "v_exp_f32 v65, v65\n\t" \
    "v_exp_f32 v66, v66\n\t" \
    "v_exp_f32 v67, v67\n\t" \
    "v_mfma_f32_32x32x16_bf16 v[96:111], v[140:143], v[128:131], v[96:111]\n\t" \
    "v_exp_f32 v68, v68\n\t" \
    "v_exp_f32 v69, v69\n\t" \
    "v_exp_f32 v70, v70\n\t" \
    "v_cvt_pk_bf16_f32 v128, v64, v65\n\t" \
    "v_exp_f32 v71, v71\n\t" \
    "v_cvt_pk_bf16_f32 v129, v66, v67\n\t" \
    "v_mfma_f32_32x32x16_bf16 v[96:111], v[144:147], v[132:135], v[96:111]\n\t" \
    "ds_read_b128 v[136:139], %[kcur] offset:2048\n\t" \
    "s_mov_b64 exec, %[vmask]\n\t" \
    "ds_read_b128 v[140:143], %[vcur] offset:0\n\t" \
    "ds_read_b128 v[144:147], %[vcur] offset:512\n\t" \
    "s_mov_b64 exec, -1\n\t" \
    "v_exp_f32 v72, v72\n\t" \
    "v_cvt_pk_bf16_f32 v130, v68, v69\n\t" \
    "v_exp_f32 v73, v73\n\t" \
    "v_cvt_pk_bf16_f32 v131, v70, v71\n\t" \
    "buffer_load_dwordx4 v[152:155], %[l16], %[srd], %[sk] offen sc1\n\t" \
    "buffer_load_dwordx4 v[156:159], %[l16], %[srd], %[sv] offen sc1\n\t" \
    "v_exp_f32 v74, v74\n\t" \
    "v_exp_f32 v75, v75\n\t" \
    "v_exp_f32 v76, v76\n\t" \
    "v_cvt_pk_bf16_f32 v132, v72, v73\n\t" \
    "v_exp_f32 v77, v77\n\t" \
    "v_cvt_pk_bf16_f32 v133, v74, v75\n\t" \
    "v_exp_f32 v78, v78\n\t" \
    "v_exp_f32 v79, v79\n\t" \
    "v_cvt_pk_bf16_f32 v134, v76, v77\n\t" \
    "v_cvt_pk_bf16_f32 v135, v78, v79\n\t" \
    "s_waitcnt lgkmcnt(0)\n\t" \
    "v_mfma_f32_32x32x16_bf16 v[64:79], v[136:139], v[148:151], v[112:127]\n\t" \
    "v_exp_f32 v80, v80\n\t" \
    "v_exp_f32 v81, v81\n\t" \
    "v_exp_f32 v82, v82\n\t" \
    "v_exp_f32 v83, v83\n\t" \
    "v_mfma_f32_32x32x16_bf16 v[96:111], v[140:143], v[128:131], v[96:111]\n\t" \
    "v_exp_f32 v84, v84\n\t" \
    "v_exp_f32 v85, v85\n\t" \
    "v_exp_f32 v86, v86\n\t" \
    "v_cvt_pk_bf16_f32 v128, v80, v81\n\t" \
    "v_exp_f32 v87, v87\n\t" \
    "v_cvt_pk_bf16_f32 v129, v82, v83\n\t" \
    "v_mfma_f32_32x32x16_bf16 v[96:111], v[144:147], v[132:135], v[96:111]\n\t" \
    "ds_read_b128 v[136:139], %[kcur] offset:3072\n\t" \
    "s_mov_b64 exec, %[vmask]\n\t" \
    "ds_read_b128 v[140:143], %[vcur] offset:1024\n\t" \
    "ds_read_b128 v[144:147], %[vcur] offset:1536\n\t" \
    "s_mov_b64 exec, -1\n\t" \
    "v_exp_f32 v88, v88\n\t" \
    "v_cvt_pk_bf16_f32 v130, v84, v85\n\t" \
    "v_exp_f32 v89, v89\n\t" \
    "v_cvt_pk_bf16_f32 v131, v86, v87\n\t" \
    "v_exp_f32 v90, v90\n\t" \
    "v_exp_f32 v91, v91\n\t" \
    "v_exp_f32 v92, v92\n\t" \
    "v_cvt_pk_bf16_f32 v132, v88, v89\n\t" \
    "v_exp_f32 v93, v93\n\t" \
    "v_cvt_pk_bf16_f32 v133, v90, v91\n\t" \
    "v_exp_f32 v94, v94\n\t" \
    "v_exp_f32 v95, v95\n\t" \
    "v_cvt_pk_bf16_f32 v134, v92, v93\n\t" \
    "v_cvt_pk_bf16_f32 v135, v94, v95\n\t" \
    "s_setprio 2\n\t" \
    "s_waitcnt lgkmcnt(0)\n\t" \
    "v_mfma_f32_32x32x16_bf16 v[80:95], v[136:139], v[148:151], v[112:127]\n\t" \
    "v_exp_f32 v64, v64\n\t" \
    "v_exp_f32 v65, v65\n\t" \
    "v_exp_f32 v66, v66\n\t" \
    "v_exp_f32 v67, v67\n\t" \
    "v_mfma_f32_32x32x16_bf16 v[96:111], v[140:143], v[128:131], v[96:111]\n\t" \
    "v_exp_f32 v68, v68\n\t" \
    "v_exp_f32 v69, v69\n\t" \
    "v_exp_f32 v70, v70\n\t" \
    "v_cvt_pk_bf16_f32 v128, v64, v65\n\t" \
    "v_exp_f32 v71, v71\n\t" \
    "v_cvt_pk_bf16_f32 v129, v66, v67\n\t" \
    "v_mfma_f32_32x32x16_bf16 v[96:111], v[144:147], v[132:135], v[96:111]\n\t" \
    "ds_read_b128 v[136:139], %[kcur] offset:4096\n\t" \
    "s_mov_b64 exec, %[vmask]\n\t" \
    "ds_read_b128 v[140:143], %[vcur] offset:2048\n\t" \
    "ds_read_b128 v[144:147], %[vcur] offset:2560\n\t" \
    "s_mov_b64 exec, -1\n\t" \
    "v_exp_f32 v72, v72\n\t" \
    "v_cvt_pk_bf16_f32 v130, v68, v69\n\t" \
    "v_exp_f32 v73, v73\n\t" \
    "v_cvt_pk_bf16_f32 v131, v70, v71\n\t" \
    "v_exp_f32 v74, v74\n\t" \
    "v_exp_f32 v75, v75\n\t" \
    "v_exp_f32 v76, v76\n\t" \
    "v_cvt_pk_bf16_f32 v132, v72, v73\n\t" \
    "v_exp_f32 v77, v77\n\t" \
    "v_cvt_pk_bf16_f32 v133, v74, v75\n\t" \
    "v_exp_f32 v78, v78\n\t" \
    "v_exp_f32 v79, v79\n\t" \
    "v_cvt_pk_bf16_f32 v134, v76, v77\n\t" \
    "v_cvt_pk_bf16_f32 v135, v78, v79\n\t" \
    "s_waitcnt lgkmcnt(0)\n\t" \
    "v_mfma_f32_32x32x16_bf16 v[64:79], v[136:139], v[148:151], v[112:127]\n\t" \
    "v_exp_f32 v80, v80\n\t" \
    "v_exp_f32 v81, v81\n\t" \
    "v_exp_f32 v82, v82\n\t" \
    "v_exp_f32 v83, v83\n\t" \
    "v_mfma_f32_32x32x16_bf16 v[96:111], v[140:143], v[128:131], v[96:111]\n\t" \
    "v_exp_f32 v84, v84\n\t" \
    "v_exp_f32 v85, v85\n\t" \
    "v_exp_f32 v86, v86\n\t" \
    "v_cvt_pk_bf16_f32 v128, v80, v81\n\t" \
    "v_exp_f32 v87, v87\n\t" \
    "v_cvt_pk_bf16_f32 v129, v82, v83\n\t" \
    "v_mfma_f32_32x32x16_bf16 v[96:111], v[144:147], v[132:135], v[96:111]\n\t" \
    "ds_read_b128 v[136:139], %[kcur] offset:5120\n\t" \
    "s_mov_b64 exec, %[vmask]\n\t" \
    "ds_read_b128 v[140:143], %[vcur] offset:3072\n\t" \
    "ds_read_b128 v[144:147], %[vcur] offset:3584\n\t" \
    "s_mov_b64 exec, -1\n\t" \
    "v_exp_f32 v88, v88\n\t" \
    "v_cvt_pk_bf16_f32 v130, v84, v85\n\t" \
    "v_exp_f32 v89, v89\n\t" \
    "v_cvt_pk_bf16_f32 v131, v86, v87\n\t" \
    "v_exp_f32 v90, v90\n\t" \
    "v_exp_f32 v91, v91\n\t" \
    "v_exp_f32 v92, v92\n\t" \
    "v_cvt_pk_bf16_f32 v132, v88, v89\n\t" \
    "v_exp_f32 v93, v93\n\t" \
    "v_cvt_pk_bf16_f32 v133, v90, v91\n\t" \
    "v_exp_f32 v94, v94\n\t" \
    "v_exp_f32 v95, v95\n\t" \
    "v_cvt_pk_bf16_f32 v134, v92, v93\n\t" \
    "v_cvt_pk_bf16_f32 v135, v94, v95\n\t" \
    "s_setprio 1\n\t" \
    "s_waitcnt lgkmcnt(0)\n\t" \
    "v_mfma_f32_32x32x16_bf16 v[80:95], v[136:139], v[148:151], v[112:127]\n\t" \
    "v_exp_f32 v64, v64\n\t" \
    "v_exp_f32 v65, v65\n\t" \
    "v_exp_f32 v66, v66\n\t" \
    "v_exp_f32 v67, v67\n\t" \
    "v_mfma_f32_32x32x16_bf16 v[96:111], v[140:143], v[128:131], v[96:111]\n\t" \
    "v_exp_f32 v68, v68\n\t" \
    "v_exp_f32 v69, v69\n\t" \
    "v_exp_f32 v70, v70\n\t" \
    "v_cvt_pk_bf16_f32 v128, v64, v65\n\t" \
    "v_exp_f32 v71, v71\n\t" \
    "v_cvt_pk_bf16_f32 v129, v66, v67\n\t" \
    "v_mfma_f32_32x32x16_bf16 v[96:111], v[144:147], v[132:135], v[96:111]\n\t" \
    "ds_read_b128 v[136:139], %[kcur] offset:6144\n\t" \
    "s_mov_b64 exec, %[vmask]\n\t" \
    "ds_read_b128 v[140:143], %[vcur] offset:4096\n\t" \
    "ds_read_b128 v[144:147], %[vcur] offset:4608\n\t" \
    "s_mov_b64 exec, -1\n\t" \
    "v_exp_f32 v72, v72\n\t" \
    "v_cvt_pk_bf16_f32 v130, v68, v69\n\t" \
    "v_exp_f32 v73, v73\n\t" \
    "v_cvt_pk_bf16_f32 v131, v70, v71\n\t" \
    "v_exp_f32 v74, v74\n\t" \
    "v_exp_f32 v75, v75\n\t" \
    "v_exp_f32 v76, v76\n\t" \
    "v_cvt_pk_bf16_f32 v132, v72, v73\n\t" \
    "v_exp_f32 v77, v77\n\t" \
    "v_cvt_pk_bf16_f32 v133, v74, v75\n\t" \
    "v_exp_f32 v78, v78\n\t" \
    "v_exp_f32 v79, v79\n\t" \
    "v_cvt_pk_bf16_f32 v134, v76, v77\n\t" \
    "v_cvt_pk_bf16_f32 v135, v78, v79\n\t" \
    "s_waitcnt lgkmcnt(0)\n\t" \
    "v_mfma_f32_32x32x16_bf16 v[64:79], v[136:139], v[148:151], v[112:127]\n\t" \
    "v_exp_f32 v80, v80\n\t" \
    "v_exp_f32 v81, v81\n\t" \
    "v_exp_f32 v82, v82\n\t" \
    "v_exp_f32 v83, v83\n\t" \
    "v_mfma_f32_32x32x16_bf16 v[96:111], v[140:143], v[128:131], v[96:111]\n\t" \
    "v_exp_f32 v84, v84\n\t" \
    "v_exp_f32 v85, v85\n\t" \
    "v_exp_f32 v86, v86\n\t" \
    "v_cvt_pk_bf16_f32 v128, v80, v81\n\t" \
    "v_exp_f32 v87, v87\n\t" \
    "v_cvt_pk_bf16_f32 v129, v82, v83\n\t" \
    "v_mfma_f32_32x32x16_bf16 v[96:111], v[144:147], v[132:135], v[96:111]\n\t" \
    "ds_read_b128 v[136:139], %[kcur] offset:7168\n\t" \
    "s_mov_b64 exec, %[vmask]\n\t" \
    "ds_read_b128 v[140:143], %[vcur] offset:5120\n\t" \
    "ds_read_b128 v[144:147], %[vcur] offset:5632\n\t" \
    "s_mov_b64 exec, -1\n\t" \
    "v_exp_f32 v88, v88\n\t" \
    "v_cvt_pk_bf16_f32 v130, v84, v85\n\t" \
    "v_exp_f32 v89, v89\n\t" \
    "v_cvt_pk_bf16_f32 v131, v86, v87\n\t" \
    "s_waitcnt vmcnt(0)\n\t" \
    "ds_write_b128 %[kvw], v[152:155]\n\t" \
    "ds_write_b128 %[kvw], v[156:159] offset:8192\n\t" \
    "v_exp_f32 v90, v90\n\t" \
    "v_exp_f32 v91, v91\n\t" \
    "v_exp_f32 v92, v92\n\t" \
    "v_cvt_pk_bf16_f32 v132, v88, v89\n\t" \
    "v_exp_f32 v93, v93\n\t" \
    "v_cvt_pk_bf16_f32 v133, v90, v91\n\t" \
    "v_exp_f32 v94, v94\n\t" \
    "v_exp_f32 v95, v95\n\t" \
    "v_cvt_pk_bf16_f32 v134, v92, v93\n\t" \
    "v_cvt_pk_bf16_f32 v135, v94, v95\n\t" \
    "s_setprio 0\n\t" \
    "s_waitcnt lgkmcnt(0)\n\t" \
    "v_mfma_f32_32x32x16_bf16 v[80:95], v[136:139], v[148:151], v[112:127]\n\t" \
    "v_exp_f32 v64, v64\n\t" \
    "v_exp_f32 v65, v65\n\t" \
    "v_exp_f32 v66, v66\n\t" \
    "v_exp_f32 v67, v67\n\t" \
    "v_mfma_f32_32x32x16_bf16 v[96:111], v[140:143], v[128:131], v[96:111]\n\t" \
    "v_exp_f32 v68, v68\n\t" \
    "v_exp_f32 v69, v69\n\t" \
    "v_exp_f32 v70, v70\n\t" \
    "v_cvt_pk_bf16_f32 v128, v64, v65\n\t" \
    "v_exp_f32 v71, v71\n\t" \
    "v_cvt_pk_bf16_f32 v129, v66, v67\n\t" \
    "v_mfma_f32_32x32x16_bf16 v[96:111], v[144:147], v[132:135], v[96:111]\n\t" \
    "ds_read_b128 v[136:139], %[knext] offset:0\n\t" \
    "s_mov_b64 exec, %[vmask]\n\t" \
    "ds_read_b128 v[140:143], %[vcur] offset:6144\n\t" \
    "ds_read_b128 v[144:147], %[vcur] offset:6656\n\t" \
    "s_mov_b64 exec, -1\n\t" \
    "v_exp_f32 v72, v72\n\t" \
    "v_cvt_pk_bf16_f32 v130, v68, v69\n\t" \
    "v_exp_f32 v73, v73\n\t" \
    "v_cvt_pk_bf16_f32 v131, v70, v71\n\t" \
    "v_exp_f32 v74, v74\n\t" \
    "v_exp_f32 v75, v75\n\t" \
    "v_exp_f32 v76, v76\n\t" \
    "v_cvt_pk_bf16_f32 v132, v72, v73\n\t" \
    "v_exp_f32 v77, v77\n\t" \
    "v_cvt_pk_bf16_f32 v133, v74, v75\n\t" \
    "v_exp_f32 v78, v78\n\t" \
    "v_exp_f32 v79, v79\n\t" \
    "v_cvt_pk_bf16_f32 v134, v76, v77\n\t" \
    "v_cvt_pk_bf16_f32 v135, v78, v79\n\t" \
    "s_waitcnt lgkmcnt(0)\n\t" \
    "v_mfma_f32_32x32x16_bf16 v[64:79], v[136:139], v[148:151], v[112:127]\n\t" \
    "v_exp_f32 v80, v80\n\t" \
    "v_exp_f32 v81, v81\n\t" \
    "v_exp_f32 v82, v82\n\t" \
    "v_exp_f32 v83, v83\n\t" \
    "v_mfma_f32_32x32x16_bf16 v[96:111], v[140:143], v[128:131], v[96:111]\n\t" \
    "v_exp_f32 v84, v84\n\t" \
    "v_exp_f32 v85, v85\n\t" \
    "v_exp_f32 v86, v86\n\t" \
    "v_cvt_pk_bf16_f32 v128, v80, v81\n\t" \
    "v_exp_f32 v87, v87\n\t" \
    "v_cvt_pk_bf16_f32 v129, v82, v83\n\t" \
    "v_mfma_f32_32x32x16_bf16 v[96:111], v[144:147], v[132:135], v[96:111]\n\t" \
    "ds_read_b128 v[136:139], %[knext] offset:1024\n\t" \
    "s_mov_b64 exec, %[vmask]\n\t" \
    "ds_read_b128 v[140:143], %[vcur] offset:7168\n\t" \
    "ds_read_b128 v[144:147], %[vcur] offset:7680\n\t" \
    "s_mov_b64 exec, -1\n\t" \
    "v_exp_f32 v88, v88\n\t" \
    "v_cvt_pk_bf16_f32 v130, v84, v85\n\t" \
    "v_exp_f32 v89, v89\n\t" \
    "v_cvt_pk_bf16_f32 v131, v86, v87\n\t" \
    "v_exp_f32 v90, v90\n\t" \
    "v_exp_f32 v91, v91\n\t" \
    "v_exp_f32 v92, v92\n\t" \
    "v_cvt_pk_bf16_f32 v132, v88, v89\n\t" \
    "v_exp_f32 v93, v93\n\t" \
    "v_cvt_pk_bf16_f32 v133, v90, v91\n\t" \
    "v_exp_f32 v94, v94\n\t" \
    "v_exp_f32 v95, v95\n\t" \
    "v_cvt_pk_bf16_f32 v134, v92, v93\n\t" \
    "v_cvt_pk_bf16_f32 v135, v94, v95\n\t" \
    "s_waitcnt vmcnt(0) lgkmcnt(0)\n\t"

#define ATTN_C5 \
    "s_nop 1\n\t" \
    "s_setprio 3\n\t" \
    "s_waitcnt lgkmcnt(0)\n\t" \
    "v_mfma_f32_32x32x16_bf16 v[80:95], v[136:139], v[148:151], v[112:127]\n\t" \
    "v_exp_f32 v64, v64\n\t" \
    "v_exp_f32 v65, v65\n\t" \
    "v_exp_f32 v66, v66\n\t" \
    "v_exp_f32 v67, v67\n\t" \
    "v_mfma_f32_32x32x16_bf16 v[96:111], v[140:143], v[128:131], v[96:111]\n\t" \
    "v_exp_f32 v68, v68\n\t" \
    "v_exp_f32 v69, v69\n\t" \
    "v_exp_f32 v70, v70\n\t" \
    "v_cvt_pk_bf16_f32 v128, v64, v65\n\t" \
    "v_exp_f32 v71, v71\n\t" \
    "v_cvt_pk_bf16_f32 v129, v66, v67\n\t" \
    "v_mfma_f32_32x32x16_bf16 v[96:111], v[144:147], v[132:135], v[96:111]\n\t" \
    "ds_read_b128 v[136:139], %[kcur] offset:2048\n\t" \
    "s_mov_b64 exec, %[vmask]\n\t" \
    "ds_read_b128 v[140:143], %[vcur] offset:0\n\t" \
    "ds_read_b128 v[144:147], %[vcur] offset:512\n\t" \
    "s_mov_b64 exec, -1\n\t" \
    "v_exp_f32 v72, v72\n\t" \
    "v_cvt_pk_bf16_f32 v130, v68, v69\n\t" \
    "v_exp_f32 v73, v73\n\t" \
    "v_cvt_pk_bf16_f32 v131, v70, v71\n\t" \
    "buffer_load_dwordx4 v[152:155], %[l16], %[srd], %[sk] offen sc1\n\t" \
    "buffer_load_dwordx4 v[156:159], %[l16], %[srd], %[sv] offen sc1\n\t" \
    "s_add_u32 %[stmp], %[hbase], 12288\n\t" \
    "buffer_load_dwordx4 v[208:211], %[l16], %[srd], %[stmp] offen sc1\n\t" \
    "s_add_u32 %[stmp], %[hbase], 13312\n\t" \
    "buffer_load_dwordx4 v[212:215], %[l16], %[srd], %[stmp] offen sc1\n\t" \
    "v_exp_f32 v74, v74\n\t" \
    "v_exp_f32 v75, v75\n\t" \
    "v_exp_f32 v76, v76\n\t" \
    "v_cvt_pk_bf16_f32 v132, v72, v73\n\t" \
    "v_exp_f32 v77, v77\n\t" \
    "v_cvt_pk_bf16_f32 v133, v74, v75\n\t" \
    "v_exp_f32 v78, v78\n\t" \
    "v_exp_f32 v79, v79\n\t" \
    "v_cvt_pk_bf16_f32 v134, v76, v77\n\t" \
    "v_cvt_pk_bf16_f32 v135, v78, v79\n\t" \
    "s_waitcnt lgkmcnt(0)\n\t" \
    "v_mfma_f32_32x32x16_bf16 v[64:79], v[136:139], v[148:151], v[112:127]\n\t" \
    "v_exp_f32 v80, v80\n\t" \
    "v_exp_f32 v81, v81\n\t" \
    "v_exp_f32 v82, v82\n\t" \
    "v_exp_f32 v83, v83\n\t" \
    "v_mfma_f32_32x32x16_bf16 v[96:111], v[140:143], v[128:131], v[96:111]\n\t" \
    "v_exp_f32 v84, v84\n\t" \
    "v_exp_f32 v85, v85\n\t" \
    "v_exp_f32 v86, v86\n\t" \
    "v_cvt_pk_bf16_f32 v128, v80, v81\n\t" \
    "v_exp_f32 v87, v87\n\t" \
    "v_cvt_pk_bf16_f32 v129, v82, v83\n\t" \
    "v_mfma_f32_32x32x16_bf16 v[96:111], v[144:147], v[132:135], v[96:111]\n\t" \
    "ds_read_b128 v[136:139], %[kcur] offset:3072\n\t" \
    "s_mov_b64 exec, %[vmask]\n\t" \
    "ds_read_b128 v[140:143], %[vcur] offset:1024\n\t" \
    "ds_read_b128 v[144:147], %[vcur] offset:1536\n\t" \
    "s_mov_b64 exec, -1\n\t" \
    "v_exp_f32 v88, v88\n\t" \
    "v_cvt_pk_bf16_f32 v130, v84, v85\n\t" \
    "v_exp_f32 v89, v89\n\t" \
    "v_cvt_pk_bf16_f32 v131, v86, v87\n\t" \
    "s_add_u32 %[stmp], %[hbase], 14336\n\t" \
    "buffer_load_dwordx4 v[216:219], %[l16], %[srd], %[stmp] offen sc1\n\t" \
    "s_add_u32 %[stmp], %[hbase], 15360\n\t" \
    "buffer_load_dwordx4 v[220:223], %[l16], %[srd], %[stmp] offen sc1\n\t" \
    "v_exp_f32 v90, v90\n\t" \
    "v_exp_f32 v91, v91\n\t" \
    "v_exp_f32 v92, v92\n\t" \
    "v_cvt_pk_bf16_f32 v132, v88, v89\n\t" \
    "v_exp_f32 v93, v93\n\t" \
    "v_cvt_pk_bf16_f32 v133, v90, v91\n\t" \
    "v_exp_f32 v94, v94\n\t" \
    "v_exp_f32 v95, v95\n\t" \
    "v_cvt_pk_bf16_f32 v134, v92, v93\n\t" \
    "v_cvt_pk_bf16_f32 v135, v94, v95\n\t" \
    "s_setprio 2\n\t" \
    "s_waitcnt lgkmcnt(0)\n\t" \
    "v_mfma_f32_32x32x16_bf16 v[80:95], v[136:139], v[148:151], v[112:127]\n\t" \
    "v_exp_f32 v64, v64\n\t" \
    "v_exp_f32 v65, v65\n\t" \
    "v_exp_f32 v66, v66\n\t" \
    "v_exp_f32 v67, v67\n\t" \
    "v_mfma_f32_32x32x16_bf16 v[96:111], v[140:143], v[128:131], v[96:111]\n\t" \
    "v_exp_f32 v68, v68\n\t" \
    "v_exp_f32 v69, v69\n\t" \
    "v_exp_f32 v70, v70\n\t" \
    "v_cvt_pk_bf16_f32 v128, v64, v65\n\t" \
    "v_exp_f32 v71, v71\n\t" \
    "v_cvt_pk_bf16_f32 v129, v66, v67\n\t" \
    "v_mfma_f32_32x32x16_bf16 v[96:111], v[144:147], v[132:135], v[96:111]\n\t" \
    "ds_read_b128 v[136:139], %[kcur] offset:4096\n\t" \
    "s_mov_b64 exec, %[vmask]\n\t" \
    "ds_read_b128 v[140:143], %[vcur] offset:2048\n\t" \
    "ds_read_b128 v[144:147], %[vcur] offset:2560\n\t" \
    "s_mov_b64 exec, -1\n\t" \
    "v_exp_f32 v72, v72\n\t" \
    "v_cvt_pk_bf16_f32 v130, v68, v69\n\t" \
    "v_exp_f32 v73, v73\n\t" \
    "v_cvt_pk_bf16_f32 v131, v70, v71\n\t" \
    "s_add_u32 %[stmp], %[hbase], 16384\n\t" \
    "buffer_load_dwordx4 v[224:227], %[l16], %[srd], %[stmp] offen sc1\n\t" \
    "s_add_u32 %[stmp], %[hbase], 17408\n\t" \
    "buffer_load_dwordx4 v[228:231], %[l16], %[srd], %[stmp] offen sc1\n\t" \
    "v_exp_f32 v74, v74\n\t" \
    "v_exp_f32 v75, v75\n\t" \
    "v_exp_f32 v76, v76\n\t" \
    "v_cvt_pk_bf16_f32 v132, v72, v73\n\t" \
    "v_exp_f32 v77, v77\n\t" \
    "v_cvt_pk_bf16_f32 v133, v74, v75\n\t" \
    "v_exp_f32 v78, v78\n\t" \
    "v_exp_f32 v79, v79\n\t" \
    "v_cvt_pk_bf16_f32 v134, v76, v77\n\t" \
    "v_cvt_pk_bf16_f32 v135, v78, v79\n\t" \
    "s_waitcnt lgkmcnt(0)\n\t" \
    "v_mfma_f32_32x32x16_bf16 v[64:79], v[136:139], v[148:151], v[112:127]\n\t" \
    "v_exp_f32 v80, v80\n\t" \
    "v_exp_f32 v81, v81\n\t" \
    "v_exp_f32 v82, v82\n\t" \
    "v_exp_f32 v83, v83\n\t" \
    "v_mfma_f32_32x32x16_bf16 v[96:111], v[140:143], v[128:131], v[96:111]\n\t" \
    "v_exp_f32 v84, v84\n\t" \
    "v_exp_f32 v85, v85\n\t" \
    "v_exp_f32 v86, v86\n\t" \
    "v_cvt_pk_bf16_f32 v128, v80, v81\n\t" \
    "v_exp_f32 v87, v87\n\t" \
    "v_cvt_pk_bf16_f32 v129, v82, v83\n\t" \
    "v_mfma_f32_32x32x16_bf16 v[96:111], v[144:147], v[132:135], v[96:111]\n\t" \
    "ds_read_b128 v[136:139], %[kcur] offset:5120\n\t" \
    "s_mov_b64 exec, %[vmask]\n\t" \
    "ds_read_b128 v[140:143], %[vcur] offset:3072\n\t" \
    "ds_read_b128 v[144:147], %[vcur] offset:3584\n\t" \
    "s_mov_b64 exec, -1\n\t" \
    "v_exp_f32 v88, v88\n\t" \
    "v_cvt_pk_bf16_f32 v130, v84, v85\n\t" \
    "v_exp_f32 v89, v89\n\t" \
    "v_cvt_pk_bf16_f32 v131, v86, v87\n\t" \
    "s_add_u32 %[stmp], %[hbase], 18432\n\t" \
    "buffer_load_dwordx4 v[232:235], %[l16], %[srd], %[stmp] offen sc1\n\t" \
    "s_add_u32 %[stmp], %[hbase], 19456\n\t" \
    "buffer_load_dwordx4 v[236:239], %[l16], %[srd], %[stmp] offen sc1\n\t" \
    "v_exp_f32 v90, v90\n\t" \
    "v_exp_f32 v91, v91\n\t" \
    "v_exp_f32 v92, v92\n\t" \
    "v_cvt_pk_bf16_f32 v132, v88, v89\n\t" \
    "v_exp_f32 v93, v93\n\t" \
    "v_cvt_pk_bf16_f32 v133, v90, v91\n\t" \
    "v_exp_f32 v94, v94\n\t" \
    "v_exp_f32 v95, v95\n\t" \
    "v_cvt_pk_bf16_f32 v134, v92, v93\n\t" \
    "v_cvt_pk_bf16_f32 v135, v94, v95\n\t" \
    "s_setprio 1\n\t" \
    "s_waitcnt lgkmcnt(0)\n\t" \
    "v_mfma_f32_32x32x16_bf16 v[80:95], v[136:139], v[148:151], v[112:127]\n\t" \
    "v_exp_f32 v64, v64\n\t" \
    "v_exp_f32 v65, v65\n\t" \
    "v_exp_f32 v66, v66\n\t" \
    "v_exp_f32 v67, v67\n\t" \
    "v_mfma_f32_32x32x16_bf16 v[96:111], v[140:143], v[128:131], v[96:111]\n\t" \
    "v_exp_f32 v68, v68\n\t" \
    "v_exp_f32 v69, v69\n\t" \
    "v_exp_f32 v70, v70\n\t" \
    "v_cvt_pk_bf16_f32 v128, v64, v65\n\t" \
    "v_exp_f32 v71, v71\n\t" \
    "v_cvt_pk_bf16_f32 v129, v66, v67\n\t" \
    "v_mfma_f32_32x32x16_bf16 v[96:111], v[144:147], v[132:135], v[96:111]\n\t" \
    "ds_read_b128 v[136:139], %[kcur] offset:6144\n\t" \
    "s_mov_b64 exec, %[vmask]\n\t" \
    "ds_read_b128 v[140:143], %[vcur] offset:4096\n\t" \
    "ds_read_b128 v[144:147], %[vcur] offset:4608\n\t" \
    "s_mov_b64 exec, -1\n\t" \
    "v_exp_f32 v72, v72\n\t" \
    "v_cvt_pk_bf16_f32 v130, v68, v69\n\t" \
    "v_exp_f32 v73, v73\n\t" \
    "v_cvt_pk_bf16_f32 v131, v70, v71\n\t" \
    "s_add_u32 %[stmp], %[hlast], 0\n\t" \
    "buffer_load_dwordx4 v[240:243], %[l16], %[srd], %[stmp] offen sc1\n\t" \
    "s_add_u32 %[stmp], %[hlast], 1024\n\t" \
    "buffer_load_dwordx4 v[244:247], %[l16], %[srd], %[stmp] offen sc1\n\t" \
    "v_exp_f32 v74, v74\n\t" \
    "v_exp_f32 v75, v75\n\t" \
    "v_exp_f32 v76, v76\n\t" \
    "v_cvt_pk_bf16_f32 v132, v72, v73\n\t" \
    "v_exp_f32 v77, v77\n\t" \
    "v_cvt_pk_bf16_f32 v133, v74, v75\n\t" \
    "v_exp_f32 v78, v78\n\t" \
    "v_exp_f32 v79, v79\n\t" \
    "v_cvt_pk_bf16_f32 v134, v76, v77\n\t" \
    "v_cvt_pk_bf16_f32 v135, v78, v79\n\t" \
    "s_waitcnt lgkmcnt(0)\n\t" \
    "v_mfma_f32_32x32x16_bf16 v[64:79], v[136:139], v[148:151], v[112:127]\n\t" \
    "v_exp_f32 v80, v80\n\t" \
    "v_exp_f32 v81, v81\n\t" \
    "v_exp_f32 v82, v82\n\t" \
    "v_exp_f32 v83, v83\n\t" \
    "v_mfma_f32_32x32x16_bf16 v[96:111], v[140:143], v[128:131], v[96:111]\n\t" \
    "v_exp_f32 v84, v84\n\t" \
    "v_exp_f32 v85, v85\n\t" \
    "v_exp_f32 v86, v86\n\t" \
    "v_cvt_pk_bf16_f32 v128, v80, v81\n\t" \
    "v_exp_f32 v87, v87\n\t" \
    "v_cvt_pk_bf16_f32 v129, v82, v83\n\t" \
    "v_mfma_f32_32x32x16_bf16 v[96:111], v[144:147], v[132:135], v[96:111]\n\t" \
    "ds_read_b128 v[136:139], %[kcur] offset:7168\n\t" \
    "s_mov_b64 exec, %[vmask]\n\t" \
    "ds_read_b128 v[140:143], %[vcur] offset:5120\n\t" \
    "ds_read_b128 v[144:147], %[vcur] offset:5632\n\t" \
    "s_mov_b64 exec, -1\n\t" \
    "v_exp_f32 v88, v88\n\t" \
    "v_cvt_pk_bf16_f32 v130, v84, v85\n\t" \
    "v_exp_f32 v89, v89\n\t" \
    "v_cvt_pk_bf16_f32 v131, v86, v87\n\t" \
    "s_waitcnt vmcnt(10)\n\t" \
    "ds_write_b128 %[kvw], v[152:155]\n\t" \
    "ds_write_b128 %[kvw], v[156:159] offset:8192\n\t" \
    "s_add_u32 m0, %[wl0], 0\n\t" \
    "s_add_u32 %[stmp], %[ws0], 196608\n\t" \
    "buffer_load_dwordx4 %[vw], %[wsrd], %[stmp] offen lds\n\t" \
    "v_exp_f32 v90, v90\n\t" \
    "v_exp_f32 v91, v91\n\t" \
    "v_exp_f32 v92, v92\n\t" \
    "v_cvt_pk_bf16_f32 v132, v88, v89\n\t" \
    "v_exp_f32 v93, v93\n\t" \
    "v_cvt_pk_bf16_f32 v133, v90, v91\n\t" \
    "v_exp_f32 v94, v94\n\t" \
    "v_exp_f32 v95, v95\n\t" \
    "v_cvt_pk_bf16_f32 v134, v92, v93\n\t" \
    "v_cvt_pk_bf16_f32 v135, v94, v95\n\t" \
    "s_setprio 0\n\t" \
    "s_waitcnt lgkmcnt(0)\n\t" \
    "v_mfma_f32_32x32x16_bf16 v[80:95], v[136:139], v[148:151], v[112:127]\n\t" \
    "v_exp_f32 v64, v64\n\t" \
    "v_exp_f32 v65, v65\n\t" \
    "v_exp_f32 v66, v66\n\t" \
    "v_exp_f32 v67, v67\n\t" \
    "v_mfma_f32_32x32x16_bf16 v[96:111], v[140:143], v[128:131], v[96:111]\n\t" \
    "v_exp_f32 v68, v68\n\t" \
    "v_exp_f32 v69, v69\n\t" \
    "v_exp_f32 v70, v70\n\t" \
    "v_cvt_pk_bf16_f32 v128, v64, v65\n\t" \
    "v_exp_f32 v71, v71\n\t" \
    "v_cvt_pk_bf16_f32 v129, v66, v67\n\t" \
    "v_mfma_f32_32x32x16_bf16 v[96:111], v[144:147], v[132:135], v[96:111]\n\t" \
    "ds_read_b128 v[136:139], %[knext] offset:0\n\t" \
    "s_mov_b64 exec, %[vmask]\n\t" \
    "ds_read_b128 v[140:143], %[vcur] offset:6144\n\t" \
    "ds_read_b128 v[144:147], %[vcur] offset:6656\n\t" \
    "s_mov_b64 exec, -1\n\t" \
    "v_exp_f32 v72, v72\n\t" \
    "v_cvt_pk_bf16_f32 v130, v68, v69\n\t" \
    "v_exp_f32 v73, v73\n\t" \
    "v_cvt_pk_bf16_f32 v131, v70, v71\n\t" \
    "s_add_u32 m0, %[wl0], 1024\n\t" \
    "s_add_u32 %[stmp], %[ws0], 204800\n\t" \
    "buffer_load_dwordx4 %[vw], %[wsrd], %[stmp] offen lds\n\t" \
    "s_add_u32 m0, %[wl0], 2048\n\t" \
    "s_add_u32 %[stmp], %[ws0], 212992\n\t" \
    "buffer_load_dwordx4 %[vw], %[wsrd], %[stmp] offen lds\n\t" \
    "v_exp_f32 v74, v74\n\t" \
    "v_exp_f32 v75, v75\n\t" \
    "v_exp_f32 v76, v76\n\t" \
    "v_cvt_pk_bf16_f32 v132, v72, v73\n\t" \
    "v_exp_f32 v77, v77\n\t" \
    "v_cvt_pk_bf16_f32 v133, v74, v75\n\t" \
    "v_exp_f32 v78, v78\n\t" \
    "v_exp_f32 v79, v79\n\t" \
    "v_cvt_pk_bf16_f32 v134, v76, v77\n\t" \
    "v_cvt_pk_bf16_f32 v135, v78, v79\n\t" \
    "s_waitcnt lgkmcnt(0)\n\t" \
    "v_mfma_f32_32x32x16_bf16 v[64:79], v[136:139], v[148:151], v[112:127]\n\t" \
    "v_exp_f32 v80, v80\n\t" \
    "v_exp_f32 v81, v81\n\t" \
    "v_exp_f32 v82, v82\n\t" \
    "v_exp_f32 v83, v83\n\t" \
    "v_mfma_f32_32x32x16_bf16 v[96:111], v[140:143], v[128:131], v[96:111]\n\t" \
    "v_exp_f32 v84, v84\n\t" \
    "v_exp_f32 v85, v85\n\t" \
    "v_exp_f32 v86, v86\n\t" \
    "v_cvt_pk_bf16_f32 v128, v80, v81\n\t" \
    "v_exp_f32 v87, v87\n\t" \
    "v_cvt_pk_bf16_f32 v129, v82, v83\n\t" \
    "v_mfma_f32_32x32x16_bf16 v[96:111], v[144:147], v[132:135], v[96:111]\n\t" \
    "ds_read_b128 v[136:139], %[knext] offset:1024\n\t" \
    "s_mov_b64 exec, %[vmask]\n\t" \
    "ds_read_b128 v[140:143], %[vcur] offset:7168\n\t" \
    "ds_read_b128 v[144:147], %[vcur] offset:7680\n\t" \
    "s_mov_b64 exec, -1\n\t" \
    "v_exp_f32 v88, v88\n\t" \
    "v_cvt_pk_bf16_f32 v130, v84, v85\n\t" \
    "v_exp_f32 v89, v89\n\t" \
    "v_cvt_pk_bf16_f32 v131, v86, v87\n\t" \
    "s_add_u32 m0, %[wl0], 3072\n\t" \
    "s_add_u32 %[stmp], %[ws0], 221184\n\t" \
    "buffer_load_dwordx4 %[vw], %[wsrd], %[stmp] offen lds\n\t" \
    "s_add_u32 m0, %[wl0], 32768\n\t" \
    "s_add_u32 %[stmp], %[ws0], 262144\n\t" \
    "buffer_load_dwordx4 %[vw], %[wsrd], %[stmp] offen lds\n\t" \
    "v_exp_f32 v90, v90\n\t" \
    "v_exp_f32 v91, v91\n\t" \
    "v_exp_f32 v92, v92\n\t" \
    "v_cvt_pk_bf16_f32 v132, v88, v89\n\t" \
    "v_exp_f32 v93, v93\n\t" \
    "v_cvt_pk_bf16_f32 v133, v90, v91\n\t" \
    "v_exp_f32 v94, v94\n\t" \
    "v_exp_f32 v95, v95\n\t" \
    "v_cvt_pk_bf16_f32 v134, v92, v93\n\t" \
    "v_cvt_pk_bf16_f32 v135, v94, v95\n\t" \
    "s_waitcnt lgkmcnt(0)\n\t"

#define ATTN_C6 \
    "s_nop 1\n\t" \
    "s_setprio 3\n\t" \
    "s_waitcnt lgkmcnt(0)\n\t" \
    "v_mfma_f32_32x32x16_bf16 v[80:95], v[136:139], v[148:151], v[112:127]\n\t" \
    "v_exp_f32 v64, v64\n\t" \
    "v_exp_f32 v65, v65\n\t" \
    "v_exp_f32 v66, v66\n\t" \
    "v_exp_f32 v67, v67\n\t" \
    "v_mfma_f32_32x32x16_bf16 v[96:111], v[140:143], v[128:131], v[96:111]\n\t" \
    "v_exp_f32 v68, v68\n\t" \
    "v_exp_f32 v69, v69\n\t" \
    "v_exp_f32 v70, v70\n\t" \
    "v_cvt_pk_bf16_f32 v128, v64, v65\n\t" \
    "v_exp_f32 v71, v71\n\t" \
    "v_cvt_pk_bf16_f32 v129, v66, v67\n\t" \
    "v_mfma_f32_32x32x16_bf16 v[96:111], v[144:147], v[132:135], v[96:111]\n\t" \
    "ds_read_b128 v[136:139], %[kcur] offset:2048\n\t" \
    "s_mov_b64 exec, %[vmask]\n\t" \
    "ds_read_b128 v[140:143], %[vcur] offset:0\n\t" \
    "ds_read_b128 v[144:147], %[vcur] offset:512\n\t" \
    "s_mov_b64 exec, -1\n\t" \
    "v_exp_f32 v72, v72\n\t" \
    "v_cvt_pk_bf16_f32 v130, v68, v69\n\t" \
    "v_exp_f32 v73, v73\n\t" \
    "v_cvt_pk_bf16_f32 v131, v70, v71\n\t" \
    "s_add_u32 m0, %[wl0], 33792\n\t" \
    "s_add_u32 %[stmp], %[ws0], 270336\n\t" \
    "buffer_load_dwordx4 %[vw], %[wsrd], %[stmp] offen lds\n\t" \
    "s_add_u32 m0, %[wl0], 34816\n\t" \
    "s_add_u32 %[stmp], %[ws0], 278528\n\t" \
    "buffer_load_dwordx4 %[vw], %[wsrd], %[stmp] offen lds\n\t" \
    "v_exp_f32 v74, v74\n\t" \
    "v_exp_f32 v75, v75\n\t" \
    "v_exp_f32 v76, v76\n\t" \
    "v_cvt_pk_bf16_f32 v132, v72, v73\n\t" \
    "v_exp_f32 v77, v77\n\t" \
    "v_cvt_pk_bf16_f32 v133, v74, v75\n\t" \
    "v_exp_f32 v78, v78\n\t" \
    "v_exp_f32 v79, v79\n\t" \
    "v_cvt_pk_bf16_f32 v134, v76, v77\n\t" \
    "v_cvt_pk_bf16_f32 v135, v78, v79\n\t" \
    "s_waitcnt lgkmcnt(0)\n\t" \
    "v_mfma_f32_32x32x16_bf16 v[64:79], v[136:139], v[148:151], v[112:127]\n\t" \
    "v_exp_f32 v80, v80\n\t" \
    "v_exp_f32 v81, v81\n\t" \
    "v_exp_f32 v82, v82\n\t" \
    "v_exp_f32 v83, v83\n\t" \
    "v_mfma_f32_32x32x16_bf16 v[96:111], v[140:143], v[128:131], v[96:111]\n\t" \
    "v_exp_f32 v84, v84\n\t" \
    "v_exp_f32 v85, v85\n\t" \
    "v_exp_f32 v86, v86\n\t" \
    "v_cvt_pk_bf16_f32 v128, v80, v81\n\t" \
    "v_exp_f32 v87, v87\n\t" \
    "v_cvt_pk_bf16_f32 v129, v82, v83\n\t" \
    "v_mfma_f32_32x32x16_bf16 v[96:111], v[144:147], v[132:135], v[96:111]\n\t" \
    "ds_read_b128 v[136:139], %[kcur] offset:3072\n\t" \
    "s_mov_b64 exec, %[vmask]\n\t" \
    "ds_read_b128 v[140:143], %[vcur] offset:1024\n\t" \
    "ds_read_b128 v[144:147], %[vcur] offset:1536\n\t" \
    "s_mov_b64 exec, -1\n\t" \
    "v_exp_f32 v88, v88\n\t" \
    "v_cvt_pk_bf16_f32 v130, v84, v85\n\t" \
    "v_exp_f32 v89, v89\n\t" \
    "v_cvt_pk_bf16_f32 v131, v86, v87\n\t" \
    "s_add_u32 m0, %[wl0], 35840\n\t" \
    "s_add_u32 %[stmp], %[ws0], 286720\n\t" \
    "buffer_load_dwordx4 %[vw], %[wsrd], %[stmp] offen lds\n\t" \
    "v_exp_f32 v90, v90\n\t" \
    "v_exp_f32 v91, v91\n\t" \
    "v_exp_f32 v92, v92\n\t" \
    "v_cvt_pk_bf16_f32 v132, v88, v89\n\t" \
    "v_exp_f32 v93, v93\n\t" \
    "v_cvt_pk_bf16_f32 v133, v90, v91\n\t" \
    "v_exp_f32 v94, v94\n\t" \
    "v_exp_f32 v95, v95\n\t" \
    "v_cvt_pk_bf16_f32 v134, v92, v93\n\t" \
    "v_cvt_pk_bf16_f32 v135, v94, v95\n\t" \
    "s_setprio 2\n\t" \
    "s_waitcnt lgkmcnt(0)\n\t" \
    "v_mfma_f32_32x32x16_bf16 v[80:95], v[136:139], v[148:151], v[112:127]\n\t" \
    "v_exp_f32 v64, v64\n\t" \
    "v_exp_f32 v65, v65\n\t" \
    "v_exp_f32 v66, v66\n\t" \
    "v_exp_f32 v67, v67\n\t" \
    "v_mfma_f32_32x32x16_bf16 v[96:111], v[140:143], v[128:131], v[96:111]\n\t" \
    "v_exp_f32 v68, v68\n\t" \
    "v_exp_f32 v69, v69\n\t" \
    "v_exp_f32 v70, v70\n\t" \
    "v_cvt_pk_bf16_f32 v128, v64, v65\n\t" \
    "v_exp_f32 v71, v71\n\t" \
    "v_cvt_pk_bf16_f32 v129, v66, v67\n\t" \
    "v_mfma_f32_32x32x16_bf16 v[96:111], v[144:147], v[132:135], v[96:111]\n\t" \
    "ds_read_b128 v[136:139], %[kcur] offset:4096\n\t" \
    "s_mov_b64 exec, %[vmask]\n\t" \
    "ds_read_b128 v[140:143], %[vcur] offset:2048\n\t" \
    "ds_read_b128 v[144:147], %[vcur] offset:2560\n\t" \
    "s_mov_b64 exec, -1\n\t" \
    "v_exp_f32 v72, v72\n\t" \
    "v_cvt_pk_bf16_f32 v130, v68, v69\n\t" \
    "v_exp_f32 v73, v73\n\t" \
    "v_cvt_pk_bf16_f32 v131, v70, v71\n\t" \
    "s_add_u32 m0, %[w5l], 0\n\t" \
    "s_add_u32 %[stmp], %[w5s], 0\n\t" \
    "buffer_load_dwordx4 %[vw], %[wsrd], %[stmp] offen lds\n\t" \
    "v_exp_f32 v74, v74\n\t" \
    "v_exp_f32 v75, v75\n\t" \
    "v_exp_f32 v76, v76\n\t" \
    "v_cvt_pk_bf16_f32 v132, v72, v73\n\t" \
    "v_exp_f32 v77, v77\n\t" \
    "v_cvt_pk_bf16_f32 v133, v74, v75\n\t" \
    "v_exp_f32 v78, v78\n\t" \
    "v_exp_f32 v79, v79\n\t" \
    "v_cvt_pk_bf16_f32 v134, v76, v77\n\t" \
    "v_cvt_pk_bf16_f32 v135, v78, v79\n\t" \
    "s_waitcnt lgkmcnt(0)\n\t" \
    "v_mfma_f32_32x32x16_bf16 v[64:79], v[136:139], v[148:151], v[112:127]\n\t" \
    "v_exp_f32 v80, v80\n\t" \
    "v_exp_f32 v81, v81\n\t" \
    "v_exp_f32 v82, v82\n\t" \
    "v_exp_f32 v83, v83\n\t" \
    "v_mfma_f32_32x32x16_bf16 v[96:111], v[140:143], v[128:131], v[96:111]\n\t" \
    "v_exp_f32 v84, v84\n\t" \
    "v_exp_f32 v85, v85\n\t" \
    "v_exp_f32 v86, v86\n\t" \
    "v_cvt_pk_bf16_f32 v128, v80, v81\n\t" \
    "v_exp_f32 v87, v87\n\t" \
    "v_cvt_pk_bf16_f32 v129, v82, v83\n\t" \
    "v_mfma_f32_32x32x16_bf16 v[96:111], v[144:147], v[132:135], v[96:111]\n\t" \
    "ds_read_b128 v[136:139], %[kcur] offset:5120\n\t" \
    "s_mov_b64 exec, %[vmask]\n\t" \
    "ds_read_b128 v[140:143], %[vcur] offset:3072\n\t" \
    "ds_read_b128 v[144:147], %[vcur] offset:3584\n\t" \
    "s_mov_b64 exec, -1\n\t" \
    "v_exp_f32 v88, v88\n\t" \
    "v_cvt_pk_bf16_f32 v130, v84, v85\n\t" \
    "v_exp_f32 v89, v89\n\t" \
    "v_cvt_pk_bf16_f32 v131, v86, v87\n\t" \
    "s_add_u32 m0, %[w5l], 1024\n\t" \
    "s_add_u32 %[stmp], %[w5s], 8192\n\t" \
    "buffer_load_dwordx4 %[vw], %[wsrd], %[stmp] offen lds\n\t" \
    "v_exp_f32 v90, v90\n\t" \
    "v_exp_f32 v91, v91\n\t" \
    "v_exp_f32 v92, v92\n\t" \
    "v_cvt_pk_bf16_f32 v132, v88, v89\n\t" \
    "v_exp_f32 v93, v93\n\t" \
    "v_cvt_pk_bf16_f32 v133, v90, v91\n\t" \
    "v_exp_f32 v94, v94\n\t" \
    "v_exp_f32 v95, v95\n\t" \
    "v_cvt_pk_bf16_f32 v134, v92, v93\n\t" \
    "v_cvt_pk_bf16_f32 v135, v94, v95\n\t" \
    "s_setprio 1\n\t" \
    "s_waitcnt lgkmcnt(0)\n\t" \
    "v_mfma_f32_32x32x16_bf16 v[80:95], v[136:139], v[148:151], v[112:127]\n\t" \
    "v_exp_f32 v64, v64\n\t" \
    "v_exp_f32 v65, v65\n\t" \
    "v_exp_f32 v66, v66\n\t" \
    "v_exp_f32 v67, v67\n\t" \
    "v_mfma_f32_32x32x16_bf16 v[96:111], v[140:143], v[128:131], v[96:111]\n\t" \
    "v_exp_f32 v68, v68\n\t" \
    "v_exp_f32 v69, v69\n\t" \
    "v_exp_f32 v70, v70\n\t" \
    "v_cvt_pk_bf16_f32 v128, v64, v65\n\t" \
    "v_exp_f32 v71, v71\n\t" \
    "v_cvt_pk_bf16_f32 v129, v66, v67\n\t" \
    "v_mfma_f32_32x32x16_bf16 v[96:111], v[144:147], v[132:135], v[96:111]\n\t" \
    "ds_read_b128 v[136:139], %[kcur] offset:6144\n\t" \
    "s_mov_b64 exec, %[vmask]\n\t" \
    "ds_read_b128 v[140:143], %[vcur] offset:4096\n\t" \
    "ds_read_b128 v[144:147], %[vcur] offset:4608\n\t" \
    "s_mov_b64 exec, -1\n\t" \
    "v_exp_f32 v72, v72\n\t" \
    "v_cvt_pk_bf16_f32 v130, v68, v69\n\t" \
    "v_exp_f32 v73, v73\n\t" \
    "v_cvt_pk_bf16_f32 v131, v70, v71\n\t" \
    "s_add_u32 m0, %[ol0], 0\n\t" \
    "s_add_u32 %[stmp], %[os0], 0\n\t" \
    "buffer_load_dwordx4 %[vw], %[osrd], %[stmp] offen lds\n\t" \
    "v_exp_f32 v74, v74\n\t" \
    "v_exp_f32 v75, v75\n\t" \
    "v_exp_f32 v76, v76\n\t" \
    "v_cvt_pk_bf16_f32 v132, v72, v73\n\t" \
    "v_exp_f32 v77, v77\n\t" \
    "v_cvt_pk_bf16_f32 v133, v74, v75\n\t" \
    "v_exp_f32 v78, v78\n\t" \
    "v_exp_f32 v79, v79\n\t" \
    "v_cvt_pk_bf16_f32 v134, v76, v77\n\t" \
    "v_cvt_pk_bf16_f32 v135, v78, v79\n\t" \
    "s_waitcnt lgkmcnt(0)\n\t" \
    "v_mfma_f32_32x32x16_bf16 v[64:79], v[136:139], v[148:151], v[112:127]\n\t" \
    "v_exp_f32 v80, v80\n\t" \
    "v_exp_f32 v81, v81\n\t" \
    "v_exp_f32 v82, v82\n\t" \
    "v_exp_f32 v83, v83\n\t" \
    "v_mfma_f32_32x32x16_bf16 v[96:111], v[140:143], v[128:131], v[96:111]\n\t" \
    "v_exp_f32 v84, v84\n\t" \
    "v_exp_f32 v85, v85\n\t" \
    "v_exp_f32 v86, v86\n\t" \
    "v_cvt_pk_bf16_f32 v128, v80, v81\n\t" \
    "v_exp_f32 v87, v87\n\t" \
    "v_cvt_pk_bf16_f32 v129, v82, v83\n\t" \
    "v_mfma_f32_32x32x16_bf16 v[96:111], v[144:147], v[132:135], v[96:111]\n\t" \
    "ds_read_b128 v[136:139], %[kcur] offset:7168\n\t" \
    "s_mov_b64 exec, %[vmask]\n\t" \
    "ds_read_b128 v[140:143], %[vcur] offset:5120\n\t" \
    "ds_read_b128 v[144:147], %[vcur] offset:5632\n\t" \
    "s_mov_b64 exec, -1\n\t" \
    "v_exp_f32 v88, v88\n\t" \
    "v_cvt_pk_bf16_f32 v130, v84, v85\n\t" \
    "v_exp_f32 v89, v89\n\t" \
    "v_cvt_pk_bf16_f32 v131, v86, v87\n\t" \
    "s_add_u32 m0, %[ol0], 1024\n\t" \
    "s_add_u32 %[stmp], %[os0], 8192\n\t" \
    "buffer_load_dwordx4 %[vw], %[osrd], %[stmp] offen lds\n\t" \
    "v_exp_f32 v90, v90\n\t" \
    "v_exp_f32 v91, v91\n\t" \
    "v_exp_f32 v92, v92\n\t" \
    "v_cvt_pk_bf16_f32 v132, v88, v89\n\t" \
    "v_exp_f32 v93, v93\n\t" \
    "v_cvt_pk_bf16_f32 v133, v90, v91\n\t" \
    "v_exp_f32 v94, v94\n\t" \
    "v_exp_f32 v95, v95\n\t" \
    "v_cvt_pk_bf16_f32 v134, v92, v93\n\t" \
    "v_cvt_pk_bf16_f32 v135, v94, v95\n\t" \
    "s_setprio 0\n\t" \
    "s_waitcnt lgkmcnt(0)\n\t" \
    "v_mfma_f32_32x32x16_bf16 v[80:95], v[136:139], v[148:151], v[112:127]\n\t" \
    "v_exp_f32 v64, v64\n\t" \
    "v_exp_f32 v65, v65\n\t" \
    "v_exp_f32 v66, v66\n\t" \
    "v_exp_f32 v67, v67\n\t" \
    "v_mfma_f32_32x32x16_bf16 v[96:111], v[140:143], v[128:131], v[96:111]\n\t" \
    "v_exp_f32 v68, v68\n\t" \
    "v_exp_f32 v69, v69\n\t" \
    "v_exp_f32 v70, v70\n\t" \
    "v_cvt_pk_bf16_f32 v128, v64, v65\n\t" \
    "v_exp_f32 v71, v71\n\t" \
    "v_cvt_pk_bf16_f32 v129, v66, v67\n\t" \
    "v_mfma_f32_32x32x16_bf16 v[96:111], v[144:147], v[132:135], v[96:111]\n\t" \
    "ds_read_b128 v[136:139], %[knext] offset:0\n\t" \
    "s_mov_b64 exec, %[vmask]\n\t" \
    "ds_read_b128 v[140:143], %[vcur] offset:6144\n\t" \
    "ds_read_b128 v[144:147], %[vcur] offset:6656\n\t" \
    "s_mov_b64 exec, -1\n\t" \
    "v_exp_f32 v72, v72\n\t" \
    "v_cvt_pk_bf16_f32 v130, v68, v69\n\t" \
    "v_exp_f32 v73, v73\n\t" \
    "v_cvt_pk_bf16_f32 v131, v70, v71\n\t" \
    "s_add_u32 m0, %[ol0], 2048\n\t" \
    "s_add_u32 %[stmp], %[os0], 16384\n\t" \
    "buffer_load_dwordx4 %[vw], %[osrd], %[stmp] offen lds\n\t" \
    "v_exp_f32 v74, v74\n\t" \
    "v_exp_f32 v75, v75\n\t" \
    "v_exp_f32 v76, v76\n\t" \
    "v_cvt_pk_bf16_f32 v132, v72, v73\n\t" \
    "v_exp_f32 v77, v77\n\t" \
    "v_cvt_pk_bf16_f32 v133, v74, v75\n\t" \
    "v_exp_f32 v78, v78\n\t" \
    "v_exp_f32 v79, v79\n\t" \
    "v_cvt_pk_bf16_f32 v134, v76, v77\n\t" \
    "v_cvt_pk_bf16_f32 v135, v78, v79\n\t" \
    "s_waitcnt lgkmcnt(0)\n\t" \
    "v_mfma_f32_32x32x16_bf16 v[64:79], v[136:139], v[148:151], v[112:127]\n\t" \
    "v_exp_f32 v80, v80\n\t" \
    "v_exp_f32 v81, v81\n\t" \
    "v_exp_f32 v82, v82\n\t" \
    "v_exp_f32 v83, v83\n\t" \
    "v_mfma_f32_32x32x16_bf16 v[96:111], v[140:143], v[128:131], v[96:111]\n\t" \
    "v_exp_f32 v84, v84\n\t" \
    "v_exp_f32 v85, v85\n\t" \
    "v_exp_f32 v86, v86\n\t" \
    "v_cvt_pk_bf16_f32 v128, v80, v81\n\t" \
    "v_exp_f32 v87, v87\n\t" \
    "v_cvt_pk_bf16_f32 v129, v82, v83\n\t" \
    "v_mfma_f32_32x32x16_bf16 v[96:111], v[144:147], v[132:135], v[96:111]\n\t" \
    "ds_read_b128 v[136:139], %[knext] offset:1024\n\t" \
    "s_mov_b64 exec, %[vmask]\n\t" \
    "ds_read_b128 v[140:143], %[vcur] offset:7168\n\t" \
    "ds_read_b128 v[144:147], %[vcur] offset:7680\n\t" \
    "s_mov_b64 exec, -1\n\t" \
    "v_exp_f32 v88, v88\n\t" \
    "v_cvt_pk_bf16_f32 v130, v84, v85\n\t" \
    "v_exp_f32 v89, v89\n\t" \
    "v_cvt_pk_bf16_f32 v131, v86, v87\n\t" \
    "s_add_u32 m0, %[ol0], 3072\n\t" \
    "s_add_u32 %[stmp], %[os0], 24576\n\t" \
    "buffer_load_dwordx4 %[vw], %[osrd], %[stmp] offen lds\n\t" \
    "v_exp_f32 v90, v90\n\t" \
    "v_exp_f32 v91, v91\n\t" \
    "v_exp_f32 v92, v92\n\t" \
    "v_cvt_pk_bf16_f32 v132, v88, v89\n\t" \
    "v_exp_f32 v93, v93\n\t" \
    "v_cvt_pk_bf16_f32 v133, v90, v91\n\t" \
    "v_exp_f32 v94, v94\n\t" \
    "v_exp_f32 v95, v95\n\t" \
    "v_cvt_pk_bf16_f32 v134, v92, v93\n\t" \
    "v_cvt_pk_bf16_f32 v135, v94, v95\n\t" \
    "s_waitcnt lgkmcnt(0)\n\t"

#define ATTN_C7 \
    "s_nop 1\n\t" \
    "s_setprio 3\n\t" \
    "s_waitcnt lgkmcnt(0)\n\t" \
    "v_mfma_f32_32x32x16_bf16 v[80:95], v[136:139], v[148:151], v[112:127]\n\t" \
    "v_exp_f32 v64, v64\n\t" \
    "v_exp_f32 v65, v65\n\t" \
    "v_exp_f32 v66, v66\n\t" \
    "v_exp_f32 v67, v67\n\t" \
    "v_mfma_f32_32x32x16_bf16 v[96:111], v[140:143], v[128:131], v[96:111]\n\t" \
    "v_exp_f32 v68, v68\n\t" \
    "v_exp_f32 v69, v69\n\t" \
    "v_exp_f32 v70, v70\n\t" \
    "v_cvt_pk_bf16_f32 v128, v64, v65\n\t" \
    "v_exp_f32 v71, v71\n\t" \
    "v_cvt_pk_bf16_f32 v129, v66, v67\n\t" \
    "v_mfma_f32_32x32x16_bf16 v[96:111], v[144:147], v[132:135], v[96:111]\n\t" \
    "ds_read_b128 v[136:139], %[kcur] offset:2048\n\t" \
    "s_mov_b64 exec, %[vmask]\n\t" \
    "ds_read_b128 v[140:143], %[vcur] offset:0\n\t" \
    "ds_read_b128 v[144:147], %[vcur] offset:512\n\t" \
    "s_mov_b64 exec, -1\n\t" \
    "v_exp_f32 v72, v72\n\t" \
    "v_cvt_pk_bf16_f32 v130, v68, v69\n\t" \
    "v_exp_f32 v73, v73\n\t" \
    "v_cvt_pk_bf16_f32 v131, v70, v71\n\t" \
    "s_add_u32 m0, %[ol0], 0\n\t" \
    "s_add_u32 %[stmp], %[os0], 0\n\t" \
    "buffer_load_dwordx4 %[vw], %[osrd], %[stmp] offen lds\n\t" \
    "v_exp_f32 v74, v74\n\t" \
    "v_exp_f32 v75, v75\n\t" \
    "v_exp_f32 v76, v76\n\t" \
    "v_cvt_pk_bf16_f32 v132, v72, v73\n\t" \
    "v_exp_f32 v77, v77\n\t" \
    "v_cvt_pk_bf16_f32 v133, v74, v75\n\t" \
    "v_exp_f32 v78, v78\n\t" \
    "v_exp_f32 v79, v79\n\t" \
    "v_cvt_pk_bf16_f32 v134, v76, v77\n\t" \
    "v_cvt_pk_bf16_f32 v135, v78, v79\n\t" \
    "s_waitcnt lgkmcnt(0)\n\t" \
    "v_mfma_f32_32x32x16_bf16 v[64:79], v[136:139], v[148:151], v[112:127]\n\t" \
    "v_exp_f32 v80, v80\n\t" \
    "v_exp_f32 v81, v81\n\t" \
    "v_exp_f32 v82, v82\n\t" \
    "v_exp_f32 v83, v83\n\t" \
    "v_mfma_f32_32x32x16_bf16 v[96:111], v[140:143], v[128:131], v[96:111]\n\t" \
    "v_exp_f32 v84, v84\n\t" \
    "v_exp_f32 v85, v85\n\t" \
    "v_exp_f32 v86, v86\n\t" \
    "v_cvt_pk_bf16_f32 v128, v80, v81\n\t" \
    "v_exp_f32 v87, v87\n\t" \
    "v_cvt_pk_bf16_f32 v129, v82, v83\n\t" \
    "v_mfma_f32_32x32x16_bf16 v[96:111], v[144:147], v[132:135], v[96:111]\n\t" \
    "ds_read_b128 v[136:139], %[kcur] offset:3072\n\t" \
    "s_mov_b64 exec, %[vmask]\n\t" \
    "ds_read_b128 v[140:143], %[vcur] offset:1024\n\t" \
    "ds_read_b128 v[144:147], %[vcur] offset:1536\n\t" \
    "s_mov_b64 exec, -1\n\t" \
    "v_exp_f32 v88, v88\n\t" \
    "v_cvt_pk_bf16_f32 v130, v84, v85\n\t" \
    "v_exp_f32 v89, v89\n\t" \
    "v_cvt_pk_bf16_f32 v131, v86, v87\n\t" \
    "s_add_u32 m0, %[ol0], 1024\n\t" \
    "s_add_u32 %[stmp], %[os0], 8192\n\t" \
    "buffer_load_dwordx4 %[vw], %[osrd], %[stmp] offen lds\n\t" \
    "v_exp_f32 v90, v90\n\t" \
    "v_exp_f32 v91, v91\n\t" \
    "v_exp_f32 v92, v92\n\t" \
    "v_cvt_pk_bf16_f32 v132, v88, v89\n\t" \
    "v_exp_f32 v93, v93\n\t" \
    "v_cvt_pk_bf16_f32 v133, v90, v91\n\t" \
    "v_exp_f32 v94, v94\n\t" \
    "v_exp_f32 v95, v95\n\t" \
    "v_cvt_pk_bf16_f32 v134, v92, v93\n\t" \
    "v_cvt_pk_bf16_f32 v135, v94, v95\n\t" \
    "s_setprio 2\n\t" \
    "s_waitcnt lgkmcnt(0)\n\t" \
    "v_mfma_f32_32x32x16_bf16 v[80:95], v[136:139], v[148:151], v[112:127]\n\t" \
    "v_exp_f32 v64, v64\n\t" \
    "v_exp_f32 v65, v65\n\t" \
    "v_exp_f32 v66, v66\n\t" \
    "v_exp_f32 v67, v67\n\t" \
    "v_mfma_f32_32x32x16_bf16 v[96:111], v[140:143], v[128:131], v[96:111]\n\t" \
    "v_exp_f32 v68, v68\n\t" \
    "v_exp_f32 v69, v69\n\t" \
    "v_exp_f32 v70, v70\n\t" \
    "v_cvt_pk_bf16_f32 v128, v64, v65\n\t" \
    "v_exp_f32 v71, v71\n\t" \
    "v_cvt_pk_bf16_f32 v129, v66, v67\n\t" \
    "v_mfma_f32_32x32x16_bf16 v[96:111], v[144:147], v[132:135], v[96:111]\n\t" \
    "ds_read_b128 v[136:139], %[kcur] offset:4096\n\t" \
    "s_mov_b64 exec, %[vmask]\n\t" \
    "ds_read_b128 v[140:143], %[vcur] offset:2048\n\t" \
    "ds_read_b128 v[144:147], %[vcur] offset:2560\n\t" \
    "s_mov_b64 exec, -1\n\t" \
    "v_exp_f32 v72, v72\n\t" \
    "v_cvt_pk_bf16_f32 v130, v68, v69\n\t" \
    "v_exp_f32 v73, v73\n\t" \
    "v_cvt_pk_bf16_f32 v131, v70, v71\n\t" \
    "v_exp_f32 v74, v74\n\t" \
    "v_exp_f32 v75, v75\n\t" \
    "v_exp_f32 v76, v76\n\t" \
    "v_cvt_pk_bf16_f32 v132, v72, v73\n\t" \
    "v_exp_f32 v77, v77\n\t" \
    "v_cvt_pk_bf16_f32 v133, v74, v75\n\t" \
    "v_exp_f32 v78, v78\n\t" \
    "v_exp_f32 v79, v79\n\t" \
    "v_cvt_pk_bf16_f32 v134, v76, v77\n\t" \
    "v_cvt_pk_bf16_f32 v135, v78, v79\n\t" \
    "s_waitcnt lgkmcnt(0)\n\t" \
    "v_mfma_f32_32x32x16_bf16 v[64:79], v[136:139], v[148:151], v[112:127]\n\t" \
    "v_exp_f32 v80, v80\n\t" \
    "v_exp_f32 v81, v81\n\t" \
    "v_exp_f32 v82, v82\n\t" \
    "v_exp_f32 v83, v83\n\t" \
    "v_mfma_f32_32x32x16_bf16 v[96:111], v[140:143], v[128:131], v[96:111]\n\t" \
    "v_exp_f32 v84, v84\n\t" \
    "v_exp_f32 v85, v85\n\t" \
    "v_exp_f32 v86, v86\n\t" \
    "v_cvt_pk_bf16_f32 v128, v80, v81\n\t" \
    "v_exp_f32 v87, v87\n\t" \
    "v_cvt_pk_bf16_f32 v129, v82, v83\n\t" \
    "v_mfma_f32_32x32x16_bf16 v[96:111], v[144:147], v[132:135], v[96:111]\n\t" \
    "ds_read_b128 v[136:139], %[kcur] offset:5120\n\t" \
    "s_mov_b64 exec, %[vmask]\n\t" \
    "ds_read_b128 v[140:143], %[vcur] offset:3072\n\t" \
    "ds_read_b128 v[144:147], %[vcur] offset:3584\n\t" \
    "s_mov_b64 exec, -1\n\t" \
    "v_exp_f32 v88, v88\n\t" \
    "v_cvt_pk_bf16_f32 v130, v84, v85\n\t" \
    "v_exp_f32 v89, v89\n\t" \
    "v_cvt_pk_bf16_f32 v131, v86, v87\n\t" \
    "v_exp_f32 v90, v90\n\t" \
    "v_exp_f32 v91, v91\n\t" \
    "v_exp_f32 v92, v92\n\t" \
    "v_cvt_pk_bf16_f32 v132, v88, v89\n\t" \
    "v_exp_f32 v93, v93\n\t" \
    "v_cvt_pk_bf16_f32 v133, v90, v91\n\t" \
    "v_exp_f32 v94, v94\n\t" \
    "v_exp_f32 v95, v95\n\t" \
    "v_cvt_pk_bf16_f32 v134, v92, v93\n\t" \
    "v_cvt_pk_bf16_f32 v135, v94, v95\n\t" \
    "s_setprio 1\n\t" \
    "s_waitcnt lgkmcnt(0)\n\t" \
    "v_mfma_f32_32x32x16_bf16 v[80:95], v[136:139], v[148:151], v[112:127]\n\t" \
    "v_exp_f32 v64, v64\n\t" \
    "v_exp_f32 v65, v65\n\t" \
    "v_exp_f32 v66, v66\n\t" \
    "v_exp_f32 v67, v67\n\t" \
    "v_mfma_f32_32x32x16_bf16 v[96:111], v[140:143], v[128:131], v[96:111]\n\t" \
    "v_exp_f32 v68, v68\n\t" \
    "v_exp_f32 v69, v69\n\t" \
    "v_exp_f32 v70, v70\n\t" \
    "v_cvt_pk_bf16_f32 v128, v64, v65\n\t" \
    "v_exp_f32 v71, v71\n\t" \
    "v_cvt_pk_bf16_f32 v129, v66, v67\n\t" \
    "v_mfma_f32_32x32x16_bf16 v[96:111], v[144:147], v[132:135], v[96:111]\n\t" \
    "ds_read_b128 v[136:139], %[kcur] offset:6144\n\t" \
    "s_mov_b64 exec, %[vmask]\n\t" \
    "ds_read_b128 v[140:143], %[vcur] offset:4096\n\t" \
    "ds_read_b128 v[144:147], %[vcur] offset:4608\n\t" \
    "s_mov_b64 exec, -1\n\t" \
    "v_exp_f32 v72, v72\n\t" \
    "v_cvt_pk_bf16_f32 v130, v68, v69\n\t" \
    "v_exp_f32 v73, v73\n\t" \
    "v_cvt_pk_bf16_f32 v131, v70, v71\n\t" \
    "v_exp_f32 v74, v74\n\t" \
    "v_exp_f32 v75, v75\n\t" \
    "v_exp_f32 v76, v76\n\t" \
    "v_cvt_pk_bf16_f32 v132, v72, v73\n\t" \
    "v_exp_f32 v77, v77\n\t" \
    "v_cvt_pk_bf16_f32 v133, v74, v75\n\t" \
    "v_exp_f32 v78, v78\n\t" \
    "v_exp_f32 v79, v79\n\t" \
    "v_cvt_pk_bf16_f32 v134, v76, v77\n\t" \
    "v_cvt_pk_bf16_f32 v135, v78, v79\n\t" \
    "s_waitcnt lgkmcnt(0)\n\t" \
    "v_mfma_f32_32x32x16_bf16 v[64:79], v[136:139], v[148:151], v[112:127]\n\t" \
    "v_exp_f32 v80, v80\n\t" \
    "v_exp_f32 v81, v81\n\t" \
    "v_exp_f32 v82, v82\n\t" \
    "v_exp_f32 v83, v83\n\t" \
    "v_mfma_f32_32x32x16_bf16 v[96:111], v[140:143], v[128:131], v[96:111]\n\t" \
    "v_exp_f32 v84, v84\n\t" \
    "v_exp_f32 v85, v85\n\t" \
    "v_exp_f32 v86, v86\n\t" \
    "v_cvt_pk_bf16_f32 v128, v80, v81\n\t" \
    "v_exp_f32 v87, v87\n\t" \
    "v_cvt_pk_bf16_f32 v129, v82, v83\n\t" \
    "v_mfma_f32_32x32x16_bf16 v[96:111], v[144:147], v[132:135], v[96:111]\n\t" \
    "ds_read_b128 v[136:139], %[kcur] offset:7168\n\t" \
    "s_mov_b64 exec, %[vmask]\n\t" \
    "ds_read_b128 v[140:143], %[vcur] offset:5120\n\t" \
    "ds_read_b128 v[144:147], %[vcur] offset:5632\n\t" \
    "s_mov_b64 exec, -1\n\t" \
    "v_exp_f32 v88, v88\n\t" \
    "v_cvt_pk_bf16_f32 v130, v84, v85\n\t" \
    "v_exp_f32 v89, v89\n\t" \
    "v_cvt_pk_bf16_f32 v131, v86, v87\n\t" \
    "v_exp_f32 v90, v90\n\t" \
    "v_exp_f32 v91, v91\n\t" \
    "v_exp_f32 v92, v92\n\t" \
    "v_cvt_pk_bf16_f32 v132, v88, v89\n\t" \
    "v_exp_f32 v93, v93\n\t" \
    "v_cvt_pk_bf16_f32 v133, v90, v91\n\t" \
    "v_exp_f32 v94, v94\n\t" \
    "v_exp_f32 v95, v95\n\t" \
    "v_cvt_pk_bf16_f32 v134, v92, v93\n\t" \
    "v_cvt_pk_bf16_f32 v135, v94, v95\n\t" \
    "s_setprio 0\n\t" \
    "s_waitcnt lgkmcnt(0)\n\t" \
    "v_mfma_f32_32x32x16_bf16 v[80:95], v[136:139], v[148:151], v[112:127]\n\t" \
    "v_exp_f32 v64, v64\n\t" \
    "v_exp_f32 v65, v65\n\t" \
    "v_exp_f32 v66, v66\n\t" \
    "v_exp_f32 v67, v67\n\t" \
    "v_mfma_f32_32x32x16_bf16 v[96:111], v[140:143], v[128:131], v[96:111]\n\t" \
    "v_exp_f32 v68, v68\n\t" \
    "v_exp_f32 v69, v69\n\t" \
    "v_exp_f32 v70, v70\n\t" \
    "v_cvt_pk_bf16_f32 v128, v64, v65\n\t" \
    "v_exp_f32 v71, v71\n\t" \
    "v_cvt_pk_bf16_f32 v129, v66, v67\n\t" \
    "v_mfma_f32_32x32x16_bf16 v[96:111], v[144:147], v[132:135], v[96:111]\n\t" \
    "ds_read_b128 v[136:139], %[knext] offset:0\n\t" \
    "s_mov_b64 exec, %[vmask]\n\t" \
    "ds_read_b128 v[140:143], %[vcur] offset:6144\n\t" \
    "ds_read_b128 v[144:147], %[vcur] offset:6656\n\t" \
    "s_mov_b64 exec, -1\n\t" \
    "v_exp_f32 v72, v72\n\t" \
    "v_cvt_pk_bf16_f32 v130, v68, v69\n\t" \
    "v_exp_f32 v73, v73\n\t" \
    "v_cvt_pk_bf16_f32 v131, v70, v71\n\t" \
    "v_exp_f32 v74, v74\n\t" \
    "v_exp_f32 v75, v75\n\t" \
    "v_exp_f32 v76, v76\n\t" \
    "v_cvt_pk_bf16_f32 v132, v72, v73\n\t" \
    "v_exp_f32 v77, v77\n\t" \
    "v_cvt_pk_bf16_f32 v133, v74, v75\n\t" \
    "v_exp_f32 v78, v78\n\t" \
    "v_exp_f32 v79, v79\n\t" \
    "v_cvt_pk_bf16_f32 v134, v76, v77\n\t" \
    "v_cvt_pk_bf16_f32 v135, v78, v79\n\t" \
    "s_waitcnt lgkmcnt(0)\n\t" \
    "v_mfma_f32_32x32x16_bf16 v[64:79], v[136:139], v[148:151], v[112:127]\n\t" \
    "v_exp_f32 v80, v80\n\t" \
    "v_exp_f32 v81, v81\n\t" \
    "v_exp_f32 v82, v82\n\t" \
    "v_exp_f32 v83, v83\n\t" \
    "v_mfma_f32_32x32x16_bf16 v[96:111], v[140:143], v[128:131], v[96:111]\n\t" \
    "v_exp_f32 v84, v84\n\t" \
    "v_exp_f32 v85, v85\n\t" \
    "v_exp_f32 v86, v86\n\t" \
    "v_cvt_pk_bf16_f32 v128, v80, v81\n\t" \
    "v_exp_f32 v87, v87\n\t" \
    "v_cvt_pk_bf16_f32 v129, v82, v83\n\t" \
    "v_mfma_f32_32x32x16_bf16 v[96:111], v[144:147], v[132:135], v[96:111]\n\t" \
    "ds_read_b128 v[136:139], %[knext] offset:1024\n\t" \
    "s_mov_b64 exec, %[vmask]\n\t" \
    "ds_read_b128 v[140:143], %[vcur] offset:7168\n\t" \
    "ds_read_b128 v[144:147], %[vcur] offset:7680\n\t" \
    "s_mov_b64 exec, -1\n\t" \
    "v_exp_f32 v88, v88\n\t" \
    "v_cvt_pk_bf16_f32 v130, v84, v85\n\t" \
    "v_exp_f32 v89, v89\n\t" \
    "v_cvt_pk_bf16_f32 v131, v86, v87\n\t" \
    "v_exp_f32 v90, v90\n\t" \
    "v_exp_f32 v91, v91\n\t" \
    "v_exp_f32 v92, v92\n\t" \
    "v_cvt_pk_bf16_f32 v132, v88, v89\n\t" \
    "v_exp_f32 v93, v93\n\t" \
    "v_cvt_pk_bf16_f32 v133, v90, v91\n\t" \
    "v_exp_f32 v94, v94\n\t" \
    "v_exp_f32 v95, v95\n\t" \
    "v_cvt_pk_bf16_f32 v134, v92, v93\n\t" \
    "v_cvt_pk_bf16_f32 v135, v94, v95\n\t" \
    "s_waitcnt vmcnt(0) lgkmcnt(0)\n\t"

template <int V> struct IC { static constexpr int value = V; };

__global__ __launch_bounds__(512) void gated_mix_kernel(const unsigned short* __restrict__ x, const unsigned short* __restrict__ wqkv,
                                                        const unsigned short* __restrict__ wo, const unsigned short* __restrict__ wg,
                                                        const unsigned short* __restrict__ wu, const unsigned short* __restrict__ wd,
                                                        const unsigned short* __restrict__ gw, unsigned short* __restrict__ out,
                                                        char* __restrict__ ws) {
    __shared__ __attribute__((aligned(1024))) char lds[LDS_BYTES];
    const int wid = __builtin_amdgcn_readfirstlane(threadIdx.x >> 6);
    const int xcd = blockIdx.x & 7, idx = blockIdx.x >> 3;
#define LANE_VALUES int lane = threadIdx.x & 63; asm volatile("" : "+v"(lane)); const int h = lane >> 5, m = lane & 31
    const unsigned lds0 = (unsigned)(uintptr_t)(lptr)lds;
    lcptr L = (lcptr)lds;
    STAMP(0);
    const srd_t sws = make_srd(ws, WS_USED);
    char* fl = ws + OFF_F;
    u64 zraw; ld8_async_sc1(zraw, fl + F_Z);
    u64 tag = 0;

    const int head = xcd * 4 + (idx & 3), qb = idx >> 2, qt = qb * 8 + wid;
    const int mb = xcd * 4 + (idx >> 3), nb = idx & 7;

    s16x8 qf;
    {
        LANE_VALUES;
        const int ty = idx & 1, j = xcd * 2 + ty, rb = idx >> 1, rot = rb & 7, mt = wid & 3, g = wid >> 2;
        const int nfb = j < 14 ? 3 : (j == 14 ? 1 : 0);
        const srd_t sx = make_srd(x, S_ * D_ * 2), sw = make_srd(wqkv, D_ * 3 * D_ * 2), sgu = make_srd(g ? wu : wg, D_ * F_ * 2);
        const int grow = rb * 128 + j * 8 + wid;
        u32x4 gx, gwv; ld16_async(gx, x + (size_t)grow * D_ + lane * 8); ld16_async(gwv, gw + lane * 8);
        const int tile = rb * 4 + mt;
        const int tq = (lane & 15) >> 2, tb = ((lane >> 4) & 1) * 32 + (lane & 3) * 8;

        auto phase12 = [&](auto Gc) {
            constexpr int G = decltype(Gc)::value;
            auto nd = [](int s) { return s >= 16 ? 0 : (s < 8 && G == 0) ? 4 : 3; };
            constexpr int NST = G == 0 ? 6 : 4;
            unsigned vq[2], vg[3];
            if constexpr (G == 0) {
#pragma unroll
                for (int t = 0; t < 2; ++t) { const int r = mt * 32 + t * 8 + (lane >> 3); vq[t] = (unsigned)((rb * 128 + r) * 1024 + (((lane & 7) ^ ((r >> 1) & 7)) << 4)); }
            } else {
                vq[0] = (unsigned)(((lane >> 2) & 7) * 3072 + (lane >> 5) * 64 + (lane & 3) * 16 + (ty * 512 + xcd * 64) * 2);
                vq[1] = (unsigned)((mt * 16 + (lane >> 2)) * 3072 + (1024 + (xcd * 4 + 2 * ty) * 16) * 2 + (lane & 3) * 16);
            }
#pragma unroll
            for (int u = 0; u < 3; ++u) { const int e = 64 * u + lane, r = (e * 5462) >> 16; vg[u] = (unsigned)(r * (F_ * 2) + (e - r * 12) * 16 + j * 192); }
            auto issue = [&](auto Sc) {
                constexpr int s = decltype(Sc)::value;
                const unsigned sl = lds0 + (s % 5) * P_SLOT; const int cc = (s + rot) & 7;
                if constexpr (s < 8) {
                    if constexpr (G == 0) {
#pragma unroll
                        for (int t = 0; t < 4; ++t) dma16(sx, vq[t & 1], cc * 128 + (t >> 1) * 16384, sl + mt * 4096 + t * 1024);
                    } else {
#pragma unroll
                        for (int u = 0; u < 2; ++u) dma16(sw, vq[0], (cc * 64 + (2 * mt + u) * 8) * 3072, sl + 16384 + (2 * mt + u) * 1024);
                        dma16(sw, vq[1], cc * 64 * 3072, sl + 24576 + mt * 1024);
                    }
                } else {
#pragma unroll
                    for (int u = 0; u < 3; ++u) dma16(sgu, vg[u], (cc * 64 + mt * 16) * (F_ * 2), sl + G * 12288 + (3 * mt + u) * 1024);
                }
            };
            unsigned xa[4], wqb[3], wgb[3];
            { const int xr = mt * 32 + m;
#pragma unroll
              for (int ks = 0; ks < 4; ++ks) xa[ks] = lds0 + (unsigned)(xr * 128 + (((2 * ks + h) ^ ((xr >> 1) & 7)) << 4)); }
            wqb[0] = lds0 + (unsigned)(G == 0 ? h * 1024 + tq * 64 + tb : (8 * h + tq) * 64 + tb);
            wgb[0] = lds0 + (unsigned)((8 * h + tq) * 192 + tb);
            wqb[1] = wqb[0] + 65536; wqb[2] = wqb[0] + 131072; wgb[1] = wgb[0] + 65536; wgb[2] = wgb[0] + 131072;
            asm volatile("" : "+v"(wqb[0]), "+v"(wqb[1]), "+v"(wqb[2]), "+v"(wgb[0]), "+v"(wgb[1]), "+v"(wgb[2]));
            auto wfrag = [&](const unsigned (&base)[3], int off, int second) {
                return lds_tr2((lcptr)(size_t)base[off >> 16] + (off & 65535), (lcptr)(size_t)base[(off + second) >> 16] + ((off + second) & 65535)); };
            s16x8 xf[32]; s16x8 wf[6];
            f32x16 a0, a1;
            f32x16 ga[3];
            auto reads = [&](auto Ic) {
                constexpr int i = decltype(Ic)::value, s = i >> 2, ks = i & 3, sl = (s % 5) * P_SLOT;
                if constexpr (s < 8) {
                    xf[i] = lds_b128((lcptr)(size_t)xa[ks] + sl);
                    if constexpr (G == 0) {
                        wf[(i % 3) * 2] = wfrag(wqb, sl + 16384 + ks * 2048, 256);
                        wf[(i % 3) * 2 + 1] = wfrag(wqb, sl + 16384 + ks * 2048 + 512, 256);
                    } else wf[(i % 3) * 2] = wfrag(wqb, sl + 24576 + ks * 1024, 256);
                } else {
#pragma unroll
                    for (int t = 0; t < 3; ++t) { const int st = 3 * G + t; wf[(i & 1) * 3 + t] = wfrag(wgb, sl + (st & 1) * 12288 + ks * 3072 + (st >> 1) * 64, 768); }
                }
            };
            u64 fq_first = 0;
#define TS(k)
            auto step = [&](auto Ic) {
                constexpr int i = decltype(Ic)::value, s = i >> 2, ks = i & 3;
                constexpr bool BAR = s < 7 ? ks == 2 : (s == 7 ? ks == 2 : (ks == 3 && s < 15));
                if constexpr (BAR) {
                    wait_vm_bar<nd(s + 2) + nd(s + 3) + ((s >= 8 && s <= 10) ? NST : 0) + (s >= 12 ? 1 : 0)>();
                    if constexpr (s == 11) { if (threadIdx.x < 2) st_flag(fl + (threadIdx.x ? F_C + qb * 256 + head * 8 : F_Q + xcd * 256 + (ty * 16 + rb) * 8), tag); }
                    TS(s + 1);
                }
                const f32x16 z = {};
                if constexpr (s < 8) {
                    if constexpr (G == 0) a0 = mfma32(wf[(i % 3) * 2], xf[i], i ? a0 : z); else a0 = mfma32(xf[i], wf[(i % 3) * 2], i ? a0 : z);
                    PIN(a0);
                } else { ga[0] = mfma32(wf[(i & 1) * 3], xf[i - 32], i > 32 ? ga[0] : z); PIN(ga[0]); }
                SBAR();
                if constexpr (G == 0) { if constexpr (BAR && s + 4 < 16) issue(IC<s + 4>{}); }
                else {
                    constexpr int ip = i - 2, sp = ip >> 2, kp = ip & 3;
                    constexpr bool BARP = ip >= 0 && (sp <= 7 ? kp == 2 : (kp == 3 && sp < 15));
                    if constexpr (BARP && sp + 4 < 16) issue(IC<sp + 4>{});
                }
                if constexpr (G == 0 ? (BAR && s == 11) : i == 49) {
                    asm volatile("s_mov_b32 m0, %3\n\ts_nop 0\n\tbuffer_load_dword %0, %1, %2 offen lds" :: "v"((unsigned)((lane < 24 ? lane : 0) * 1024 + nb * 128)),
                                 "s"(make_srd(wd, F_ * D_ * 2)), "s"((unsigned)(((wid >> 1) * 352 + (wid & 1) * 96 + (idx >> 3) * 24) * 1024)), "s"(lds0 + 5 * P_SLOT) : "memory");
                }
                if constexpr (i == 58 && G == 0) { if (wid == 0) fq_first = ld_flag(fl + F_Q + xcd * 256 + (lane & 31) * 8); }
                if constexpr (i < 30) reads(IC<i + 2>{}); else if constexpr (i >= 31 && i + 1 < 64) reads(IC<i + 1>{});
                SBAR();
                if constexpr (s < 8) {
                    if constexpr (G == 0) { a1 = mfma32(wf[(i % 3) * 2 + 1], xf[i], i ? a1 : z); PIN(a1); }
                } else {
                    ga[1] = mfma32(wf[(i & 1) * 3 + 1], xf[i - 32], i > 32 ? ga[1] : z); ga[2] = mfma32(wf[(i & 1) * 3 + 2], xf[i - 32], i > 32 ? ga[2] : z);
                    PIN(ga[1]); PIN(ga[2]);
                }
                SBAR();
                if constexpr (i == 31) STAMP(2);
                if constexpr (i == 32 || i == 33) {
                    if constexpr (G == 0) {
                        const float sc = ty ? 1.f : 0.25f * 1.4426950408889634f;
                        const unsigned base = (ty ? OFF_K : OFF_Q) + (unsigned)((xcd * 4 * 64 + tile) * 1024) + (i - 32) * 131072;
#pragma unroll
                        for (int hh = 0; hh < 2; ++hh) st_sc1(pack8(i == 32 ? a0 : a1, hh * 8, sc), sws, lane * 16, base + hh * 65536);
                    } else st_sc1(pack8(a0, (i - 32) * 8, 1.f), sws, (unsigned)((m >> 4) * 65536 + h * 256 + (m & 15) * 16),
                                  OFF_V + (unsigned)(((xcd * 4 + 2 * ty) * 64 + tile) * 1024 + (i - 32) * 512));
                    SBAR();
                }
                if constexpr (i == 34) {
                    asm volatile("" : "+v"(gx), "+v"(gwv), "+v"(zraw));
                    tag = ((u64)(unsigned)__builtin_amdgcn_readfirstlane((unsigned)(zraw >> 32)) << 32 | (unsigned)__builtin_amdgcn_readfirstlane((unsigned)zraw)) + 1;
                    float sg = 0.f;
#pragma unroll
                    for (int q = 0; q < 4; ++q)
                        sg += __uint_as_float(gx[q] << 16) * __uint_as_float(gwv[q] << 16) + __uint_as_float(gx[q] & 0xffff0000u) * __uint_as_float(gwv[q] & 0xffff0000u);
                    sg = wave_sum63(sg);
                    if (lane == 63) __hip_atomic_store((__attribute__((address_space(1))) float*)(ws + OFF_G) + grow, __builtin_amdgcn_rcpf(1.f + __expf(-sg)), __ATOMIC_RELAXED, __HIP_MEMORY_SCOPE_AGENT);
                    st_sc1((u32x4){~0u, ~0u, ~0u, ~0u}, sws, lane * 16, OFF_O + (unsigned)((qt * 32 + head) * 1024));
                    SBAR();
                }
            };
            issue(IC<0>{});
            asm volatile("s_barrier" ::: "memory");
            issue(IC<1>{}); issue(IC<2>{}); issue(IC<3>{});
            wait_vm_bar<nd(1) + nd(2) + nd(3)>();
            STAMP(1);
            TS(0);
            reads(IC<0>{}); reads(IC<1>{});
#define S4(b) step(IC<(b)>{}); step(IC<(b) + 1>{}); step(IC<(b) + 2>{}); step(IC<(b) + 3>{});
#define S16(b) S4(b) S4((b) + 4) S4((b) + 8) S4((b) + 12)
            S16(0) S16(16) S16(32) S16(48)
#undef S16
#undef S4
            STAMP(3);
            TS(16);
            {
                lptr xch = (lptr)lds + 2 * P_SLOT + wid * 2048 + lane * 16;
#pragma unroll
                for (int q = 0; q < 2; ++q) {
                    f32x4 v;
#pragma unroll
                    for (int e = 0; e < 4; ++e) v[e] = G == 0 ? ga[2][8 + 4 * q + e] : ga[0][4 * q + e];
                    *(__attribute__((address_space(3))) f32x4*)(xch + q * 1024) = v;
                }
            }
            if constexpr (G == 0) { if (wid == 0 && !__all(fq_first == tag)) poll_flags(fl + F_Q + xcd * 256, 32, tag, lane); }
            lgkm_bar();
            u32x4 kvs[6];
            qf = __builtin_bit_cast(s16x8, ld_sc1(sws, lane * 16, OFF_Q + (unsigned)((head * 64 + qt) * 1024)));
#pragma unroll
            for (int c = 0; c < 3; ++c) {
                kvs[2 * c] = ld_sc1(sws, lane * 16, OFF_K + (unsigned)((head * 64 + c * 8 + wid) * 1024));
                kvs[2 * c + 1] = ld_sc1(sws, lane * 16, OFF_V + (unsigned)((head * 64 + c * 8 + wid) * 1024));
            }
            f32x4 oth[2];
#pragma unroll
            for (int q = 0; q < 2; ++q) oth[q] = *(const __attribute__((address_space(3))) f32x4*)(L + 2 * P_SLOT + (wid ^ 4) * 2048 + q * 1024 + lane * 16);
            SBAR();
            auto hpack = [&](auto gate_of, auto up_of) {
                u32x4 r;
#pragma unroll
                for (int q = 0; q < 4; ++q) r[q] = geglu2((f32x2){gate_of(2 * q), gate_of(2 * q + 1)}, (f32x2){up_of(2 * q), up_of(2 * q + 1)});
                return r;
            };
            u32x4 h0, h1, h2;
            if constexpr (G == 0) {
                h0 = hpack([&](int e) { return ga[0][e]; }, [&](int e) { return ga[1][e]; });
                h1 = hpack([&](int e) { return ga[0][8 + e]; }, [&](int e) { return ga[1][8 + e]; });
                h2 = hpack([&](int e) { return ga[2][e]; }, [&](int e) { return oth[e >> 2][e & 3]; });
            } else {
                h0 = hpack([&](int e) { return ga[1][e]; }, [&](int e) { return ga[2][e]; });
                h1 = hpack([&](int e) { return ga[1][8 + e]; }, [&](int e) { return ga[2][8 + e]; });
                h2 = hpack([&](int e) { return oth[e >> 2][e & 3]; }, [&](int e) { return ga[0][8 + e]; });
            }
            PIN(h0); PIN(h1); PIN(h2);
            SBAR();
#pragma unroll
            for (int c = 0; c < 3; ++c) {
                lds_w128((lptr)lds + KV_BASE + c * KV_SLOT + wid * 1024 + lane * 16, kvs[2 * c]);
                lds_w128((lptr)lds + KV_BASE + c * KV_SLOT + 8192 + wid * 1024 + lane * 16, kvs[2 * c + 1]);
            }
            SBAR();
            const unsigned hb = OFF_H + (unsigned)((tile * 86 + j * 6) * 1024);
            const int fbo = G == 0 ? 0 : 2;
            if (fbo < nfb) { st_sc1(h0, sws, lane * 16, hb + fbo * 2048); st_sc1(h1, sws, lane * 16, hb + fbo * 2048 + 1024); }
            if (1 < nfb) st_sc1(h2, sws, lane * 16, hb + 2048 + G * 1024);
            asm volatile("; end of role %0" :: "i"(G) : "memory");
        };
        if (g == 0) phase12(IC<0>{}); else phase12(IC<1>{});
        lgkm_bar();
    }
    STAMP(4);

    LANE_VALUES;
    const int mi = wid & 1, kq = wid >> 1;
    const srd_t swd = make_srd(wd, F_ * D_ * 2), swo = make_srd(wo, D_ * D_ * 2);
    const unsigned vwd = (unsigned)(((lane >> 2) & 7) * 1024 + nb * 128 + (lane >> 5) * 64 + (lane & 3) * 16);
    const unsigned ws0 = (unsigned)((kq * 352 + mi * 32) * 1024), wl0 = lds0 + (unsigned)(kq * 8192 + mi * 4096);
    auto wo_piece = [&](int q, int p) { dma16(swo, vwd, (unsigned)(q * 128 * 1024) + (p >> 3) * 65536 + (p & 7) * 8192, lds0 + (unsigned)(q * 8192) + (3 + (p >> 3)) * W_SLOT + (p & 7) * 1024); };
    const unsigned kvo = OFF_K + (unsigned)((head * 64 + wid) * 1024);
    const unsigned l16 = lane * 16;
    const unsigned klane = lds0 + KV_BASE + l16, vlane = lds0 + KV_BASE + 8192 + h * 256 + (m & 15) * 16, wlane = klane + wid * 1024;
    const u64 vmask = 0x0000FFFF0000FFFFull;
    f32x16 sA, sB, o = {}, cneg; u32x8 p = {}; u32x4 kf, v0, v1, stk, stv;
    {
        const s16x8 k0 = lds_b128(L + KV_BASE + lane * 16);
        f32x16 z = {}; sA = mfma32(k0, qf, z);
        const float m0 = rowmax16(sA);
#pragma unroll
        for (int i = 0; i < 16; ++i) { cneg[i] = -m0; sA[i] -= m0; }
        kf = __builtin_bit_cast(u32x4, lds_b128(L + KV_BASE + 1024 + lane * 16));
        const unsigned one = (m == 16 || m == 20) ? 0x3f803f80u : 0u;
        v0 = (u32x4){one, one, one, one}; v1 = v0;
    }
#define STATE "+{v[64:79]}"(sA), "=&{v[80:95]}"(sB), "+{v[96:111]}"(o), "+{v[128:135]}"(p), "+{v[136:139]}"(kf), "+{v[140:143]}"(v0), "+{v[144:147]}"(v1)
#define INPUT "{v[112:127]}"(cneg), "{v[148:151]}"(qf), [kcur] "v"(kc), [knext] "v"(kn), [vcur] "v"(vc), [vmask] "s"(vmask)
#define STAGE_IN [l16] "v"(l16), [srd] "s"(sws), [sk] "s"(sk), [sv] "s"(sv), [kvw] "v"(kvw)
#define CHUNK_HEAD(c) const int ns = slot == 2 ? 0 : slot + 1, fs = ns == 2 ? 0 : ns + 1; \
        const unsigned kc = klane + slot * KV_SLOT, kn = klane + ns * KV_SLOT, vc = vlane + slot * KV_SLOT, kvw = wlane + fs * KV_SLOT; \
        const unsigned sk = kvo + ((c) + 2) * 8192, sv = sk + (2u << 20); (void)kvw; (void)sv
    int slot = 0;
    unsigned stmp;
#define TA(k)
    TA(0);
    u32x4 hf[22];
    f32x16 aB[2], aA[2];
    unsigned wa4b[3];
    wa4b[0] = lds0 + (unsigned)(kq * 8192 + (4 * h + ((lane & 15) >> 2)) * 64 + ((lane >> 4) & 1) * 32 + (lane & 3) * 8);
    wa4b[1] = wa4b[0] + 65536; wa4b[2] = wa4b[0] + 131072;
    u32x4 of[8];
    auto wstage = [&](auto Sc) {
        constexpr int s = decltype(Sc)::value, nk = s == 5 ? 2 : 4;
        lcptr sl = (lcptr)(size_t)(wa4b[(s < 6 ? s % 3 : s - 3) >> 1]) + ((s < 6 ? s % 3 : s - 3) & 1) * W_SLOT;
        s16x8 w[4][2];
#pragma unroll
        for (int ks = 0; ks < nk; ++ks)
#pragma unroll
            for (int n = 0; n < 2; ++n) w[ks][n] = lds_tr2(sl + ks * 2048 + n * 512, sl + ks * 2048 + 1024 + n * 512);
        SBAR();
#pragma unroll
        for (int ks = 0; ks < nk; ++ks)
#pragma unroll
            for (int n = 0; n < 2; ++n) {
                const f32x16 z = {};
                if constexpr (s < 6) aB[n] = mfma32(w[ks][n], __builtin_bit_cast(s16x8, hf[4 * s + ks]), (s || ks) ? aB[n] : z);
                else aA[n] = mfma32(w[ks][n], __builtin_bit_cast(s16x8, of[4 * (s - 6) + ks]), (s > 6 || ks) ? aA[n] : z);
            }
        if constexpr (s < 6) { PIN(aB[0]); PIN(aB[1]); } else { PIN(aA[0]); PIN(aA[1]); }
    };

    const unsigned hbase = OFF_H + (unsigned)(((mb * 2 + mi) * 86 + kq * 22) * 1024), hlast = kq < 3 ? hbase + 20 * 1024 : OOB;
#define HF_A(c) c "{v[160:163]}"(hf[0]), c "{v[164:167]}"(hf[1]), c "{v[168:171]}"(hf[2]), c "{v[172:175]}"(hf[3]), c "{v[176:179]}"(hf[4]), c "{v[180:183]}"(hf[5]), \
        c "{v[184:187]}"(hf[6]), c "{v[188:191]}"(hf[7]), c "{v[192:195]}"(hf[8]), c "{v[196:199]}"(hf[9]), c "{v[200:203]}"(hf[10]), c "{v[204:207]}"(hf[11])
#define HF_B(c) c "{v[208:211]}"(hf[12]), c "{v[212:215]}"(hf[13]), c "{v[216:219]}"(hf[14]), c "{v[220:223]}"(hf[15]), c "{v[224:227]}"(hf[16]), \
        c "{v[228:231]}"(hf[17]), c "{v[232:235]}"(hf[18]), c "{v[236:239]}"(hf[19]), c "{v[240:243]}"(hf[20]), c "{v[244:247]}"(hf[21])
#define STAGE_OUT "=&{v[152:155]}"(stk), "=&{v[156:159]}"(stv)
#define W_IN [vw] "v"(vwd), [wsrd] "s"(swd), [ws0] "s"(ws0), [wl0] "s"(wl0)
    {
        CHUNK_HEAD(0);
        asm volatile(ATTN_C0 : STATE, [stmp] "=&s"(stmp) : INPUT, W_IN : "memory", "scc");
        TA(1); lgkm_bar(); TA(2);
        slot = ns;
    }
    {
        CHUNK_HEAD(1);
        asm volatile(ATTN_C1 : STATE, STAGE_OUT, [stmp] "=&s"(stmp) : INPUT, STAGE_IN, W_IN : "memory", "scc");
        TA(3); lgkm_bar(); TA(4);
        if (threadIdx.x == 0) st_flag(fl + F_H + (idx >> 1) * 256 + (xcd * 2 + (idx & 1)) * 8, tag);
        slot = ns;
    }
    {
        CHUNK_HEAD(2);
        u64 fh, fc;
        const char* fhp = fl + F_H + (mb >> 1) * 256 + (lane & 15) * 8; const char* fcp = fl + F_C + (mb >> 2) * 256 + (lane & 31) * 8;
        asm volatile(ATTN_C2 : STATE, STAGE_OUT, "=&{v[248:249]}"(fh), "=&{v[250:251]}"(fc) : INPUT, STAGE_IN, [fhp] "v"(fhp), [fcp] "v"(fcp) : "memory");
        if (wid == 0) {
            if (!__all(fh == tag)) poll_flags(fl + F_H + (mb >> 1) * 256, 16, tag, lane);
            if (!__all(fc == tag)) poll_flags(fl + F_C + (mb >> 2) * 256, 32, tag, lane);
        }
        TA(5); lgkm_bar(); TA(6);
        slot = ns;
    }
    {
        CHUNK_HEAD(3);
        const unsigned vwm = (unsigned)((lane < 20 ? lane : 0) * 1024 + nb * 128), wms = (unsigned)((kq * 352 + 192 + (idx >> 3) * 40 + mi * 20) * 1024), dml = lds0 + 163840 - 256;
        asm volatile(ATTN_C3 : STATE, STAGE_OUT, [stmp] "=&s"(stmp), HF_A("=&")
                     : INPUT, STAGE_IN, [hbase] "s"(hbase), [vwm] "v"(vwm), [wsrd] "s"(swd), [wms] "s"(wms), [dml] "s"(dml) : "memory", "scc");
        TA(7); lgkm_bar(); TA(8);
        slot = ns;
    }
    {
        CHUNK_HEAD(4);
        asm volatile(ATTN_C4 : STATE, STAGE_OUT, HF_A("+") : INPUT, STAGE_IN : "memory");
        TA(9); lgkm_bar();
        slot = ns;
    }
    wstage(IC<0>{}); wstage(IC<1>{}); wstage(IC<2>{});
    asm volatile("s_barrier" ::: "memory");
    TA(10);
    {
        CHUNK_HEAD(5);
        asm volatile(ATTN_C5 : STATE, STAGE_OUT, [stmp] "=&s"(stmp), HF_B("=&")
                     : INPUT, STAGE_IN, W_IN, [hbase] "s"(hbase), [hlast] "s"(hlast) : "memory", "scc");
        TA(11); lgkm_bar(); TA(12);
        slot = ns;
    }
    {
        CHUNK_HEAD(6);
        const unsigned w5s = (unsigned)(kq * 352 * 1024 + mi * 16384 + 5 * 65536), w5l = lds0 + (unsigned)(2 * W_SLOT + kq * 8192 + mi * 2048);
        const unsigned os0 = (unsigned)((kq * 128 + mi * 32) * 1024 + 65536), ol0 = wl0 + 4 * W_SLOT;
        asm volatile(ATTN_C6 : STATE, [stmp] "=&s"(stmp), HF_B("+")
                     : INPUT, W_IN, [w5s] "s"(w5s), [w5l] "s"(w5l), [osrd] "s"(swo), [os0] "s"(os0), [ol0] "s"(ol0) : "memory", "scc");
        TA(13);
        asm volatile("s_barrier" ::: "memory");
        TA(14);
        slot = ns;
    }
    {
        CHUNK_HEAD(7);
        const unsigned os0 = (unsigned)((wid >> 2) * 131072 + (wid & 3) * 16384), ol0 = lds0 + (unsigned)(3 * W_SLOT + (wid >> 2) * 8192 + (wid & 3) * 2048);
        asm volatile(ATTN_C7 : STATE, [stmp] "=&s"(stmp), HF_B("+") : INPUT, [vw] "v"(vwd), [osrd] "s"(swo), [os0] "s"(os0), [ol0] "s"(ol0) : "memory", "scc");
    }
    TA(15);
    const float gate = __hip_atomic_load((__attribute__((address_space(1))) float*)(ws + OFF_G) + mb * 64 + mi * 32 + m, __ATOMIC_RELAXED, __HIP_MEMORY_SCOPE_AGENT);
    o = mfma32(__builtin_bit_cast(s16x8, v0), __builtin_bit_cast(s16x8, __builtin_shufflevector(p, p, 0, 1, 2, 3)), o);
    o = mfma32(__builtin_bit_cast(s16x8, v1), __builtin_bit_cast(s16x8, __builtin_shufflevector(p, p, 4, 5, 6, 7)), o);
    STAMP(5);
    float l = o[8];
    bool bad = !(l <= 1.8446744e19f);
#pragma unroll
    for (int i = 0; i < 8; ++i) bad |= !(__builtin_fabsf(o[i]) <= 3.0e38f);
    const bool wave_bad = __any(bad);
    auto store_o = [&]() {
        u32x4 ov = pack8(o, 0, __builtin_amdgcn_rcpf(l));
#pragma unroll
        for (int i = 0; i < 4; ++i) ov[i] = ov[i] == ~0u ? 0x7fc07fc0u : ov[i];
        st_sc1(ov, sws, lane * 16, OFF_O + (unsigned)((qt * 32 + head) * 1024));
    };
    if (!wave_bad) store_o();
    *(__attribute__((address_space(3))) unsigned*)((lptr)lds + CTL + wid * 4) = wave_bad ? 1u : 0u;
    lgkm_bar();
    const u32x4 ra = *(const __attribute__((address_space(3))) u32x4*)(L + CTL), rb4 = *(const __attribute__((address_space(3))) u32x4*)(L + CTL + 16);
    const unsigned redo = ra[0] | ra[1] | ra[2] | ra[3] | rb4[0] | rb4[1] | rb4[2] | rb4[3];
    if (__builtin_amdgcn_readfirstlane(redo)) {
        wait_vm_bar<0>();
        float mrun = -INFINITY;
#pragma unroll
        for (int i = 0; i < 16; ++i) o[i] = 0.f;
        const unsigned one = (m == 16 || m == 20) ? 0x3f803f80u : 0u;
        for (int c = 0; c < 8; ++c) {
            const u32x4 a = ld_sc1(sws, lane * 16, kvo + c * 8192), b = ld_sc1(sws, lane * 16, kvo + (2u << 20) + c * 8192);
            lgkm_bar();
            lds_w128((lptr)lds + KV_BASE + wid * 1024 + lane * 16, a);
            lds_w128((lptr)lds + KV_BASE + 8192 + wid * 1024 + lane * 16, b);
            lgkm_bar();
            for (int t = 0; t < 8; ++t) {
                f32x16 z = {};
                f32x16 s = mfma32(lds_b128(L + KV_BASE + t * 1024 + lane * 16), qf, z);
                const float mn = fmaxf(mrun, rowmax16(s)), f = EXP2(mrun - mn);
                mrun = mn;
#pragma unroll
                for (int i = 0; i < 16; ++i) { o[i] *= f; s[i] = EXP2(s[i] - mn); }
                const u32x4 q0 = pack8(s, 0, 1.f), q1 = pack8(s, 8, 1.f);
                s16x8 va = lds_b128(L + KV_BASE + 8192 + t * 1024 + h * 256 + (m & 15) * 16), vb = lds_b128(L + KV_BASE + 8192 + t * 1024 + 512 + h * 256 + (m & 15) * 16);
                if (m >= 16) { va = __builtin_bit_cast(s16x8, (u32x4){one, one, one, one}); vb = va; }
                o = mfma32(va, __builtin_bit_cast(s16x8, q0), o);
                o = mfma32(vb, __builtin_bit_cast(s16x8, q1), o);
            }
        }
        l = o[8];
        if (wave_bad) store_o();
        lgkm_bar();
#pragma unroll
        for (int u = 0; u < 2; ++u) wo_piece(wid >> 2, (wid & 3) * 2 + u);
    }
#pragma unroll
    for (int u = 0; u < 2; ++u) wo_piece(2 + (wid >> 2), (wid & 3) * 2 + u);
    TA(16);
    wstage(IC<3>{}); wstage(IC<4>{});
    TA(17);
    wstage(IC<5>{});
    TA(18);
    const unsigned obase = OFF_O + (unsigned)(((mb * 2 + mi) * 32 + kq * 8) * 1024);
#pragma unroll
    for (int i = 0; i < 8; ++i) of[i] = ld_sc1(sws, lane * 16, obase + i * 1024);
    TA(19);
    STAMP(6);
    for (unsigned spins = 0; spins < (1u << 17); ++spins) {
        bool all_ok = true;
#pragma unroll
        for (int i = 0; i < 8; ++i) {
            const unsigned mx = max(max(of[i][0], of[i][1]), max(of[i][2], of[i][3]));
            if (__any(mx == ~0u)) { all_ok = false; of[i] = ld_sc1(sws, lane * 16, obase + i * 1024); }
        }
        if (all_ok) break;
    }
    TA(20);
    wait_vm_bar<0>();
    STAMP(7);
    TA(21);
    wstage(IC<6>{}); wstage(IC<7>{});
    TA(22);
    const float g1 = 1.f - gate;
#pragma unroll
    for (int n = 0; n < 2; ++n)
#pragma unroll
        for (int q = 0; q < 4; ++q) {
            f32x4 v;
#pragma unroll
            for (int e = 0; e < 4; ++e) v[e] = gate * aA[n][4 * q + e] + g1 * aB[n][4 * q + e];
            *(__attribute__((address_space(3))) f32x4*)((lptr)lds + wid * 8192 + (n * 4 + q) * 1024 + lane * 16) = v;
        }
    lgkm_bar();
    if (blockIdx.x == 0 && threadIdx.x == 0) st_flag(fl + F_Z, tag);
    {
        const int rmi = wid & 1, rn = (wid >> 1) & 1, rh = wid >> 2;
        f32x4 s0 = {}, s1 = {};
#pragma unroll
        for (int k = 0; k < 4; ++k) {
            lcptr src = L + (k * 2 + rmi) * 8192 + (rn * 4 + rh * 2) * 1024 + lane * 16;
            s0 += *(const __attribute__((address_space(3))) f32x4*)src; s1 += *(const __attribute__((address_space(3))) f32x4*)(src + 1024);
        }
        const unsigned a0 = pack2(s0[0], s0[1]), a1 = pack2(s0[2], s0[3]), b0 = pack2(s1[0], s1[1]), b1 = pack2(s1[2], s1[3]);
        auto r0 = __builtin_amdgcn_permlane32_swap(a0, b0, false, false);
        auto r1 = __builtin_amdgcn_permlane32_swap(a1, b1, false, false);
        const u32x4 v = {r0[0], r1[0], r0[1], r1[1]};
        *(u32x4*)(out + (size_t)(mb * 64 + rmi * 32 + m) * D_ + nb * 64 + rn * 32 + rh * 16 + h * 8) = v;
    }
    STAMP(8);
}

extern "C" void kernel_launch(void* const* d_in, const int* in_sizes, int n_in, void* d_out, int out_size, void* d_ws, size_t ws_size,
                              hipStream_t stream) {
    gated_mix_kernel<<<256, 512, 0, stream>>>((const unsigned short*)d_in[0], (const unsigned short*)d_in[1], (const unsigned short*)d_in[2],
                                              (const unsigned short*)d_in[4], (const unsigned short*)d_in[5], (const unsigned short*)d_in[6],
                                              (const unsigned short*)d_in[7], (unsigned short*)d_out, (char*)d_ws);
}
```
